```python
import torch

def _softmax(x, axis=-1):
    return torch.softmax(x, dim=axis)
def _gelu(x):
    return torch.nn.functional.gelu(x, approximate="tanh")
def _silu(x):
    return torch.nn.functional.silu(x)
def _sigmoid(x):
    return torch.sigmoid(x)
def _layernorm(x, axis=-1, eps=1e-5):
    m = x.mean(dim=axis, keepdim=True)
    v = x.var(dim=axis, keepdim=True, unbiased=False)
    return (x - m) / torch.sqrt(v + eps)
def _scatter_add(tgt, idx, src, axis=0):
    return tgt.clone().index_add_(axis, idx, src)
def _segment_sum(src, seg, n):
    out = torch.zeros((n,) + src.shape[1:], dtype=src.dtype)
    return out.index_add_(0, seg, src)
def _pad(x, pw):
    flat = tuple(v for pair in reversed(pw) for v in pair)
    return torch.nn.functional.pad(x, flat)

kernel_name = 'parallel_sum__gqa_block_cached__glu_gated_reglu__decode'

def setup_inputs():

    _w = (lambda a: (lambda t: t.to(torch.bfloat16) if t.is_floating_point() else t)(torch.as_tensor(a)))
    def _A_setup():
        B, Sq, D = 8, 1, 512
        Hq, Hkv, Sk, Dh = 32, 32, 4096, 128
        assert Hq % Hkv == 0, (Hq, Hkv)
        return {
            "x": _w((lambda *s: torch.randn(*s, dtype=torch.bfloat16) * 0.05)(B, Sq, D)),
            "k_cache": _w((lambda *s: torch.randn(*s, dtype=torch.bfloat16) * 1.6)(B, Hkv, Sk, Dh)),
            "v_cache": _w((lambda *s: torch.randn(*s, dtype=torch.bfloat16) * 0.8)(B, Hkv, Sk, Dh)),
            "wq": _w((lambda *s: torch.randn(*s, dtype=torch.bfloat16) * 1.6)(D, Hq * Dh) * (D ** -0.5)),
            "wo": _w((lambda *s: torch.randn(*s, dtype=torch.bfloat16) * 0.8)(Hq * Dh, D) * ((Hq * Dh) ** -0.5)),
        }
    _A_in = _A_setup()

    def _B_setup():
        B, S, D, F = 8, 1, 512, 1376
        return {
            "x": _w((lambda *s: torch.randn(*s, dtype=torch.bfloat16) * 0.05)(B, S, D)),
            "W_gate": _w((lambda *s: torch.randn(*s, dtype=torch.bfloat16) * 0.4)(D, F) * (2.0 / D) ** 0.5),
            "W_up": _w((lambda *s: torch.randn(*s, dtype=torch.bfloat16) * 0.4)(D, F) * (2.0 / D) ** 0.5),
            "W_down": _w((lambda *s: torch.randn(*s, dtype=torch.bfloat16) * 0.4)(F, D) * (1.0 / F) ** 0.5),
        }
    _B_in = _B_setup()

    return {
        "A_x": _A_in["x"],
        "A_k_cache": _A_in["k_cache"],
        "A_v_cache": _A_in["v_cache"],
        "A_wq": _A_in["wq"],
        "A_wo": _A_in["wo"],
        "B_x": _B_in["x"],
        "B_W_gate": _B_in["W_gate"],
        "B_W_up": _B_in["W_up"],
        "B_W_down": _B_in["W_down"],
    }

def reference(A_x, A_k_cache, A_v_cache, A_wq, A_wo, B_x, B_W_gate, B_W_up, B_W_down):
    def _A_ref(x, k_cache, v_cache, wq, wo):
        B, Sq, D = x.shape
        Hq, Dh = 32, 128
        Hkv = k_cache.shape[1]
        rep = Hq // Hkv
        q = (lambda x, *d: x.permute(*d))((x @ wq).reshape(B, Sq, Hq, Dh), 0, 2, 1, 3)
        k = (lambda x, n, axis: torch.repeat_interleave(x, n, dim=axis))(k_cache, rep, axis=1)
        v = (lambda x, n, axis: torch.repeat_interleave(x, n, dim=axis))(v_cache, rep, axis=1)
        scores = torch.einsum("bhqd,bhkd->bhqk", q, k) * (Dh ** -0.5)
        p = _softmax(scores, axis=-1)
        o = torch.einsum("bhqk,bhkd->bhqd", p, v)
        return (lambda x, *d: x.permute(*d))(o, 0, 2, 1, 3).reshape(B, Sq, Hq * Dh) @ wo

    def _B_ref(x, W_gate, W_up, W_down):
        gate = torch.einsum("bsd,df->bsf", x, W_gate)
        up = torch.einsum("bsd,df->bsf", x, W_up)
        gate = gate * (lambda x, dt: x.to(dt))(gate > 0, gate.dtype)
        h = gate * up
        return torch.einsum("bsf,fd->bsd", h, W_down)

    x = A_x
    xn = _layernorm(x, axis=-1)
    return x + _A_ref(xn, A_k_cache, A_v_cache, A_wq, A_wo) + _B_ref(xn, B_W_gate, B_W_up, B_W_down)
```

```cpp
#include <hip/hip_runtime.h>
#include <hip/hip_bf16.h>
#include <stdint.h>

typedef unsigned int u32;
typedef __attribute__((ext_vector_type(4))) unsigned int u32x4;
typedef __attribute__((ext_vector_type(4))) float f32x4;
typedef __attribute__((ext_vector_type(2))) __bf16 bf16x2;
typedef __hip_bfloat16 bf16;

constexpr int NB = 8, DM = 512, NH = 32, SK = 4096, DH = 128, FF = 1376;
constexpr int NW = 8;
constexpr int U = 4;
constexpr int NT = (NW + 1) * 64;
constexpr int TILES = SK / (NW * 4 * U);
static_assert(TILES >= 4 && (TILES % 2) == 0, "tiles");
constexpr int WL = 128 / NW;

#define MIMIC_SCORE_ROUND 1

__device__ __forceinline__ float bf_lo(u32 x) { return __uint_as_float(x << 16); }
__device__ __forceinline__ float bf_hi(u32 x) { return __uint_as_float(x & 0xffff0000u); }
__device__ __forceinline__ float rbf(float x) { return __bfloat162float(__float2bfloat16(x)); }
__device__ __forceinline__ u32 pack_bf(float lo, float hi) {
  return (u32)__bfloat16_as_ushort(__float2bfloat16(lo)) | ((u32)__bfloat16_as_ushort(__float2bfloat16(hi)) << 16);
}
__device__ __forceinline__ float dot2(u32 a, u32 b, float acc) {
  return __builtin_amdgcn_fdot2_f32_bf16(__builtin_bit_cast(bf16x2, a), __builtin_bit_cast(bf16x2, b), acc, false);
}
template <int CTRL> __device__ __forceinline__ float dpp(float x) {
  return __builtin_bit_cast(float, __builtin_amdgcn_mov_dpp(__builtin_bit_cast(int, x), CTRL, 0xf, 0xf, true));
}
__device__ __forceinline__ float row16_sum(float x) {
  x += dpp<0xB1>(x);
  x += dpp<0x4E>(x);
  x += dpp<0x141>(x);
  x += dpp<0x140>(x);
  return x;
}
__device__ __forceinline__ float wave_sum(float x) {
  x = row16_sum(x);
  x += __shfl_xor(x, 16);
  x += __shfl_xor(x, 32);
  return x;
}
__device__ __forceinline__ u32x4 ld_nt(const bf16* p) { return __builtin_nontemporal_load(reinterpret_cast<const u32x4*>(p)); }
__device__ __forceinline__ u32x4 ld16(const bf16* p) { return *reinterpret_cast<const u32x4*>(p); }

constexpr int L_XN = 0;
constexpr int L_QP = L_XN + DM;
constexpr int L_AC = L_QP + NW * DH;
constexpr int L_ML = L_AC + NW * DH;
constexpr int L_HB = L_ML + NW * 2;
constexpr int L_YP = L_HB + 48;
constexpr int L_END = L_YP + NW * DM;

struct Set { u32x4 r[2 * U]; };

__device__ __forceinline__ void load_kv(Set& s, const bf16* kp, const bf16* vp) {
#pragma unroll
  for (int u = 0; u < U; ++u) { s.r[u] = ld_nt(kp + u * 4 * DH); s.r[U + u] = ld_nt(vp + u * 4 * DH); }
}

__device__ __forceinline__ void compute_tile(const Set& s, const u32x4& q, float& m, float& l, float (&acc)[8]) {
  constexpr float LOG2E = 1.4426950408889634f;
  float sc[U];
#pragma unroll
  for (int u = 0; u < U; ++u) {
    float d = dot2(s.r[u][0], q[0], 0.f);
    d = dot2(s.r[u][1], q[1], d);
    d = dot2(s.r[u][2], q[2], d);
    d = dot2(s.r[u][3], q[3], d);
    d = row16_sum(d);
    d = rbf(rbf(d) * 0.08838834764831845f) * LOG2E;
    sc[u] = d;
  }
  float tm = sc[0];
#pragma unroll
  for (int u = 1; u < U; ++u) tm = fmaxf(tm, sc[u]);
  const float mn = fmaxf(m, tm);
  const float alpha = __builtin_amdgcn_exp2f(m - mn);
  m = mn;
  float ps = 0.f;
#pragma unroll
  for (int u = 0; u < U; ++u) { sc[u] = __builtin_amdgcn_exp2f(sc[u] - mn); ps += sc[u]; }
  l = fmaf(l, alpha, ps);
#pragma unroll
  for (int k = 0; k < 8; ++k) acc[k] *= alpha;
#pragma unroll
  for (int u = 0; u < U; ++u) {
#pragma unroll
    for (int k = 0; k < 4; ++k) {
      acc[2 * k] = fmaf(sc[u], bf_lo(s.r[U + u][k]), acc[2 * k]);
      acc[2 * k + 1] = fmaf(sc[u], bf_hi(s.r[U + u][k]), acc[2 * k + 1]);
    }
  }
}

__device__ __forceinline__ void layernorm_to_lds(const u32x4& xv, float* lds_xn, int lane) {
  float xf[8];
#pragma unroll
  for (int k = 0; k < 4; ++k) { xf[2 * k] = bf_lo(xv[k]); xf[2 * k + 1] = bf_hi(xv[k]); }
  float s = 0.f;
#pragma unroll
  for (int k = 0; k < 8; ++k) s += xf[k];
  const float mean = wave_sum(s) * (1.f / DM);
  float s2 = 0.f;
#pragma unroll
  for (int k = 0; k < 8; ++k) { const float d = xf[k] - mean; s2 = fmaf(d, d, s2); }
  const float var = wave_sum(s2) * (1.f / DM);
  const float mb = rbf(mean);
  const float den = rbf(sqrtf(rbf(rbf(var) + 1e-5f)));
  f32x4 o0, o1;
#pragma unroll
  for (int k = 0; k < 4; ++k) { o0[k] = rbf(rbf(xf[k] - mb) / den); o1[k] = rbf(rbf(xf[4 + k] - mb) / den); }
  *reinterpret_cast<f32x4*>(lds_xn + 8 * lane) = o0;
  *reinterpret_cast<f32x4*>(lds_xn + 8 * lane + 4) = o1;
  __builtin_amdgcn_s_waitcnt(0xc07f);
  __builtin_amdgcn_wave_barrier();
}

__global__ void __launch_bounds__(NT) attn_ffn_main(const bf16* __restrict__ x, const bf16* __restrict__ kc,
                                                    const bf16* __restrict__ vc, const bf16* __restrict__ wq,
                                                    const bf16* __restrict__ wo, const bf16* __restrict__ wg,
                                                    const bf16* __restrict__ wu, const bf16* __restrict__ wd,
                                                    float* __restrict__ yA, float* __restrict__ yB) {
  __shared__ __attribute__((aligned(16))) float lds[L_END];
  const int id = blockIdx.x, b = id >> 5, h = id & 31;
  const int tid = threadIdx.x, lane = tid & 63;
  const int wave = __builtin_amdgcn_readfirstlane(tid >> 6);
  const int r = lane >> 4, c = lane & 15;

  if (wave == NW) {
    layernorm_to_lds(ld16(x + b * DM + 8 * lane), lds + L_XN, lane);
    const int ncg = h < 12 ? 6 : 5;
    const int cg0 = h < 12 ? 6 * h : 72 + 5 * (h - 12);
    const int cg = lane & 7, rg = lane >> 3;
    const int col = 8 * (cg0 + (cg < ncg ? cg : 0));
    float ag[8], au[8];
#pragma unroll
    for (int k = 0; k < 8; ++k) { ag[k] = 0.f; au[k] = 0.f; }
    bool synced = false;
#pragma unroll 1
    for (int p0 = 0; p0 < 64; p0 += 8) {
      u32x4 g[8], uu[8];
#pragma unroll
      for (int i = 0; i < 8; ++i) {
        const int row = rg + 8 * (p0 + i);
        g[i] = ld16(wg + (size_t)row * FF + col);
        uu[i] = ld16(wu + (size_t)row * FF + col);
      }
      if (!synced) { __syncthreads(); synced = true; }
#pragma unroll
      for (int i = 0; i < 8; ++i) {
        const float xr = lds[L_XN + rg + 8 * (p0 + i)];
#pragma unroll
        for (int k = 0; k < 4; ++k) {
          ag[2 * k] = fmaf(xr, bf_lo(g[i][k]), ag[2 * k]); ag[2 * k + 1] = fmaf(xr, bf_hi(g[i][k]), ag[2 * k + 1]);
          au[2 * k] = fmaf(xr, bf_lo(uu[i][k]), au[2 * k]); au[2 * k + 1] = fmaf(xr, bf_hi(uu[i][k]), au[2 * k + 1]);
        }
      }
    }
#pragma unroll
    for (int k = 0; k < 8; ++k) {
      ag[k] += __shfl_xor(ag[k], 8); ag[k] += __shfl_xor(ag[k], 16); ag[k] += __shfl_xor(ag[k], 32);
      au[k] += __shfl_xor(au[k], 8); au[k] += __shfl_xor(au[k], 16); au[k] += __shfl_xor(au[k], 32);
    }
    if (rg == 0 && cg < 6) {
#pragma unroll
      for (int k = 0; k < 8; ++k) {
        float gg = rbf(ag[k]);
        gg = gg > 0.f ? gg : 0.f;
        const float hv = rbf(gg * rbf(au[k]));
        lds[L_HB + 8 * cg + k] = cg < ncg ? hv : 0.f;
      }
    }
    __builtin_amdgcn_s_waitcnt(0xc07f);
    __builtin_amdgcn_wave_barrier();
    float y[8];
#pragma unroll
    for (int k = 0; k < 8; ++k) y[k] = 0.f;
    const int f0 = 8 * cg0, nf = 8 * ncg;
#pragma unroll 1
    for (int p0 = 0; p0 < 48; p0 += 16) {
      u32x4 w[16];
#pragma unroll
      for (int i = 0; i < 16; ++i) {
        const int f = p0 + i;
        w[i] = ld16(wd + (size_t)(f0 + (f < nf ? f : 0)) * DM + 8 * lane);
      }
#pragma unroll
      for (int i = 0; i < 16; ++i) {
        const float hv = lds[L_HB + p0 + i];
#pragma unroll
        for (int k = 0; k < 4; ++k) {
          y[2 * k] = fmaf(hv, bf_lo(w[i][k]), y[2 * k]); y[2 * k + 1] = fmaf(hv, bf_hi(w[i][k]), y[2 * k + 1]);
        }
      }
    }
    float* yp = yB + (size_t)id * DM + 8 * lane;
    *reinterpret_cast<f32x4*>(yp) = f32x4{y[0], y[1], y[2], y[3]};
    *reinterpret_cast<f32x4*>(yp + 4) = f32x4{y[4], y[5], y[6], y[7]};
    __syncthreads();
    __syncthreads();
    return;
  }

  const u32x4 xv = ld16(x + b * DM + 8 * lane);
  Set A, Bs;
  u32x4 W[WL];
  const bf16* kp = kc + (size_t)id * (SK * DH) + (size_t)wave * (4 * U * DH) + r * DH + 8 * c;
  const bf16* vp = vc + (size_t)id * (SK * DH) + (size_t)wave * (4 * U * DH) + r * DH + 8 * c;
  constexpr int TSTRIDE = NW * 4 * U * DH;
  {
    const bf16* wqp = wq + (size_t)(wave * 4 + r) * (NH * DH) + h * DH + 8 * c;
#pragma unroll
    for (int i = 0; i < WL; ++i) W[i] = ld16(wqp + (size_t)i * (NW * 4) * (NH * DH));
    load_kv(Bs, kp, vp);
    load_kv(A, kp + TSTRIDE, vp + TSTRIDE);
  }

  layernorm_to_lds(xv, lds + L_XN, lane);
  u32x4 q;
  {
    float qa[8];
#pragma unroll
    for (int k = 0; k < 8; ++k) qa[k] = 0.f;
#pragma unroll
    for (int i = 0; i < WL; ++i) {
      const float xr = lds[L_XN + wave * 4 + r + i * (NW * 4)];
#pragma unroll
      for (int k = 0; k < 4; ++k) {
        qa[2 * k] = fmaf(xr, bf_lo(W[i][k]), qa[2 * k]);
        qa[2 * k + 1] = fmaf(xr, bf_hi(W[i][k]), qa[2 * k + 1]);
      }
    }
#pragma unroll
    for (int k = 0; k < 8; ++k) { qa[k] += __shfl_xor(qa[k], 16); qa[k] += __shfl_xor(qa[k], 32); }
    if (r == 0) {
      *reinterpret_cast<f32x4*>(lds + L_QP + wave * DH + 8 * c) = f32x4{qa[0], qa[1], qa[2], qa[3]};
      *reinterpret_cast<f32x4*>(lds + L_QP + wave * DH + 8 * c + 4) = f32x4{qa[4], qa[5], qa[6], qa[7]};
    }
    __syncthreads();
    f32x4 s0 = {0.f, 0.f, 0.f, 0.f}, s1 = {0.f, 0.f, 0.f, 0.f};
#pragma unroll
    for (int w = 0; w < NW; ++w) {
      s0 += *reinterpret_cast<const f32x4*>(lds + L_QP + w * DH + 8 * c);
      s1 += *reinterpret_cast<const f32x4*>(lds + L_QP + w * DH + 8 * c + 4);
    }
    q[0] = pack_bf(s0[0], s0[1]); q[1] = pack_bf(s0[2], s0[3]);
    q[2] = pack_bf(s1[0], s1[1]); q[3] = pack_bf(s1[2], s1[3]);
  }

  float m = -INFINITY, l = 0.f, acc[8];
#pragma unroll
  for (int k = 0; k < 8; ++k) acc[k] = 0.f;
#pragma unroll 1
  for (int t = 0; t < TILES - 2; t += 2) {
    compute_tile(Bs, q, m, l, acc);
    load_kv(Bs, kp + (size_t)(t + 2) * TSTRIDE, vp + (size_t)(t + 2) * TSTRIDE);
    compute_tile(A, q, m, l, acc);
    load_kv(A, kp + (size_t)(t + 3) * TSTRIDE, vp + (size_t)(t + 3) * TSTRIDE);
  }
  {
    const bf16* wop = wo + (size_t)(h * DH + wave * WL) * DM + 8 * lane;
#pragma unroll
    for (int i = 0; i < WL; ++i) W[i] = ld16(wop + i * DM);
  }
  compute_tile(Bs, q, m, l, acc);
  compute_tile(A, q, m, l, acc);

  {
    float mw = fmaxf(m, __shfl_xor(m, 16));
    mw = fmaxf(mw, __shfl_xor(mw, 32));
    const float sc = __builtin_amdgcn_exp2f(m - mw);
    l *= sc;
    l += __shfl_xor(l, 16); l += __shfl_xor(l, 32);
#pragma unroll
    for (int k = 0; k < 8; ++k) { float a = acc[k] * sc; a += __shfl_xor(a, 16); a += __shfl_xor(a, 32); acc[k] = a; }
    if (r == 0) {
      *reinterpret_cast<f32x4*>(lds + L_AC + wave * DH + 8 * c) = f32x4{acc[0], acc[1], acc[2], acc[3]};
      *reinterpret_cast<f32x4*>(lds + L_AC + wave * DH + 8 * c + 4) = f32x4{acc[4], acc[5], acc[6], acc[7]};
      if (c == 0) { lds[L_ML + 2 * wave] = mw; lds[L_ML + 2 * wave + 1] = l; }
    }
  }
  __syncthreads();
  float y[8];
  {
    float M = lds[L_ML];
#pragma unroll
    for (int w = 1; w < NW; ++w) M = fmaxf(M, lds[L_ML + 2 * w]);
    float L = 0.f, o = 0.f;
    const int d = wave * WL + (lane & (WL - 1));
#pragma unroll
    for (int w = 0; w < NW; ++w) {
      const float e = __builtin_amdgcn_exp2f(lds[L_ML + 2 * w] - M);
      L = fmaf(e, lds[L_ML + 2 * w + 1], L);
      o = fmaf(e, lds[L_AC + w * DH + d], o);
    }
    o = rbf(o / L);
#pragma unroll
    for (int k = 0; k < 8; ++k) y[k] = 0.f;
#pragma unroll
    for (int i = 0; i < WL; ++i) {
      const float oi = __shfl(o, i);
#pragma unroll
      for (int k = 0; k < 4; ++k) {
        y[2 * k] = fmaf(oi, bf_lo(W[i][k]), y[2 * k]);
        y[2 * k + 1] = fmaf(oi, bf_hi(W[i][k]), y[2 * k + 1]);
      }
    }
    *reinterpret_cast<f32x4*>(lds + L_YP + wave * DM + 8 * lane) = f32x4{y[0], y[1], y[2], y[3]};
    *reinterpret_cast<f32x4*>(lds + L_YP + wave * DM + 8 * lane + 4) = f32x4{y[4], y[5], y[6], y[7]};
  }
  __syncthreads();
  {
    float s = 0.f;
#pragma unroll
    for (int w = 0; w < NW; ++w) s += lds[L_YP + w * DM + tid];
    yA[(size_t)id * DM + tid] = s;
  }
}

__global__ void __launch_bounds__(256) final_sum(const bf16* __restrict__ x, const float* __restrict__ yA,
                                                 const float* __restrict__ yB, bf16* __restrict__ out) {
  __shared__ __attribute__((aligned(16))) float red[16][64];
  const int b = blockIdx.x >> 3, cb = blockIdx.x & 7;
  const int tid = threadIdx.x, c4 = tid & 15, g = tid >> 4;
  const float* src = (g < 8 ? yA : yB) + (size_t)(b * NH + (g & 7) * 4) * DM + cb * 64 + 4 * c4;
  f32x4 v0 = *reinterpret_cast<const f32x4*>(src);
  f32x4 v1 = *reinterpret_cast<const f32x4*>(src + DM);
  f32x4 v2 = *reinterpret_cast<const f32x4*>(src + 2 * DM);
  f32x4 v3 = *reinterpret_cast<const f32x4*>(src + 3 * DM);
  *reinterpret_cast<f32x4*>(&red[g][4 * c4]) = (v0 + v1) + (v2 + v3);
  __syncthreads();
  if (tid < 64) {
    float a = 0.f, f = 0.f;
#pragma unroll
    for (int i = 0; i < 8; ++i) { a += red[i][tid]; f += red[8 + i][tid]; }
    const int n = b * DM + cb * 64 + tid;
    const float xa = rbf(__bfloat162float(x[n]) + rbf(a));
    out[n] = __float2bfloat16(xa + rbf(f));
  }
}

extern "C" void kernel_launch(void* const* d_in, const int* in_sizes, int n_in,
                              void* d_out, int out_size, void* d_ws, size_t ws_size,
                              hipStream_t stream) {
  const bf16* x = (const bf16*)d_in[0];
  const bf16* kc = (const bf16*)d_in[1];
  const bf16* vc = (const bf16*)d_in[2];
  const bf16* wq = (const bf16*)d_in[3];
  const bf16* wo = (const bf16*)d_in[4];
  const bf16* wg = (const bf16*)d_in[6];
  const bf16* wu = (const bf16*)d_in[7];
  const bf16* wd = (const bf16*)d_in[8];
  float* yA = (float*)d_ws;
  float* yB = yA + NB * NH * DM;
  attn_ffn_main<<<NB * NH, NT, 0, stream>>>(x, kc, vc, wq, wo, wg, wu, wd, yA, yB);
  final_sum<<<NB * 8, 256, 0, stream>>>(x, yA, yB, (bf16*)d_out);
}
```

```cpp
#include <hip/hip_runtime.h>
#include <hip/hip_bf16.h>
#include <stdint.h>

typedef unsigned int u32;
typedef __attribute__((ext_vector_type(4))) unsigned int u32x4;
typedef __attribute__((ext_vector_type(4))) float f32x4;
typedef __attribute__((ext_vector_type(2))) __bf16 bf16x2;
typedef __hip_bfloat16 bf16;

constexpr int NB = 8, DM = 512, NH = 32, SK = 4096, DH = 128, FF = 1376;
constexpr int NW = 8;
#define UU 4
constexpr int U = UU;
constexpr int NFW = 4;
constexpr int NT = (NW + NFW) * 64;
constexpr int TILES = SK / (NW * 4 * U);
constexpr int XK = 64;
constexpr int UF = XK / (NW * 4);
constexpr int XT = NB * XK / (NW * 4 * U);
static_assert(TILES >= 4 + 2 * XT && (TILES % 2) == 0 && (XT % 2) == 0 && XT * NW * 4 * U == NB * XK, "tiles");
constexpr int WL = 128 / NW;

#define MIMIC_SCORE_ROUND 1

__device__ __forceinline__ float bf_lo(u32 x) { return __uint_as_float(x << 16); }
__device__ __forceinline__ float bf_hi(u32 x) { return __uint_as_float(x & 0xffff0000u); }
__device__ __forceinline__ float rbf(float x) { return __bfloat162float(__float2bfloat16(x)); }
__device__ __forceinline__ u32 pack_bf(float lo, float hi) {
  return (u32)__bfloat16_as_ushort(__float2bfloat16(lo)) | ((u32)__bfloat16_as_ushort(__float2bfloat16(hi)) << 16);
}
__device__ __forceinline__ float dot2(u32 a, u32 b, float acc) {
  return __builtin_amdgcn_fdot2_f32_bf16(__builtin_bit_cast(bf16x2, a), __builtin_bit_cast(bf16x2, b), acc, false);
}
template <int CTRL> __device__ __forceinline__ float dpp(float x) {
  return __builtin_bit_cast(float, __builtin_amdgcn_mov_dpp(__builtin_bit_cast(int, x), CTRL, 0xf, 0xf, true));
}
__device__ __forceinline__ float row16_sum(float x) {
  x += dpp<0xB1>(x);
  x += dpp<0x4E>(x);
  x += dpp<0x141>(x);
  x += dpp<0x140>(x);
  return x;
}
__device__ __forceinline__ float xrow_sum(float x) {
  auto s = __builtin_amdgcn_permlane16_swap(__float_as_uint(x), __float_as_uint(x), false, false);
  x = __uint_as_float(s[0]) + __uint_as_float(s[1]);
  auto t = __builtin_amdgcn_permlane32_swap(__float_as_uint(x), __float_as_uint(x), false, false);
  return __uint_as_float(t[0]) + __uint_as_float(t[1]);
}
__device__ __forceinline__ float xrow_max(float x) {
  auto s = __builtin_amdgcn_permlane16_swap(__float_as_uint(x), __float_as_uint(x), false, false);
  x = fmaxf(__uint_as_float(s[0]), __uint_as_float(s[1]));
  auto t = __builtin_amdgcn_permlane32_swap(__float_as_uint(x), __float_as_uint(x), false, false);
  return fmaxf(__uint_as_float(t[0]), __uint_as_float(t[1]));
}
__device__ __forceinline__ float wave_sum(float x) { return xrow_sum(row16_sum(x)); }
__device__ __forceinline__ u32x4 ld_nt(const bf16* p) { return __builtin_nontemporal_load(reinterpret_cast<const u32x4*>(p)); }
__device__ __forceinline__ u32x4 ld16(const bf16* p) { return *reinterpret_cast<const u32x4*>(p); }
#define WQ_NT 1
#define WO_NT 1
#define FFN_NT 1
__device__ __forceinline__ u32x4 ld_wq(const bf16* p) { return WQ_NT ? ld_nt(p) : ld16(p); }
__device__ __forceinline__ u32x4 ld_wo(const bf16* p) { return WO_NT ? ld_nt(p) : ld16(p); }
__device__ __forceinline__ u32x4 ld_ffn(const bf16* base, u32 boff) {
  return __builtin_nontemporal_load(reinterpret_cast<const u32x4*>(reinterpret_cast<const char*>(base) + boff));
}

#define ST_MODE 0
__device__ __forceinline__ void st_part(float* p, f32x4 v) {
  *reinterpret_cast<f32x4*>(p) = v;
}
constexpr int L_WO = 0;
constexpr int L_XN = L_WO + DH * DM / 2;
constexpr int L_QP = L_XN + DM;
constexpr int L_UP = L_XN;
constexpr int L_AC = L_UP + NFW * NB * 64;
constexpr int L_ML = L_AC + NW * DH;
constexpr int L_O = L_ML + NW * 2;
constexpr int L_HB = L_O + 2 * DH;
constexpr int L_FL = L_HB + 32 * NB;
constexpr int L_PG = L_FL + 8;
constexpr int L_XA = L_PG + 8;
constexpr int L_GP = L_XA + 256 * NB;
constexpr int L_END = L_GP + NFW * NB * 64;
static_assert(L_QP + NW * DH <= L_AC, "LDS");
constexpr int L_XN2 = L_XA, L_QP2 = L_XN2 + DM, L_AC2 = L_QP2 + NW * DH, L_ML2 = L_AC2 + NW * DH;
static_assert(L_ML2 + NW * 2 <= L_END && L_END * 4 <= 163840 && (L_XA % 4) == 0 && (L_FL % 4) == 0 && (L_O % 4) == 0, "LDS");
constexpr int NFG = (FF + 63) / 64;
#define LAS __attribute__((address_space(3)))

__device__ __forceinline__ void glds16_nt(const void* gsrc, unsigned lds_dst) {
  unsigned keep;
  asm volatile("s_mov_b32 %0, m0\n\ts_mov_b32 m0, %2\n\ts_nop 0\n\tglobal_load_lds_dwordx4 %1, off nt\n\ts_mov_b32 m0, %0"
               : "=&s"(keep) : "v"(gsrc), "s"(lds_dst) : "memory");
}

#define STAMP(i)
#define PIN16(a, o) asm volatile("" : "+v"(a[o]), "+v"(a[o + 1]), "+v"(a[o + 2]), "+v"(a[o + 3]), "+v"(a[o + 4]), "+v"(a[o + 5]), "+v"(a[o + 6]), \
  "+v"(a[o + 7]), "+v"(a[o + 8]), "+v"(a[o + 9]), "+v"(a[o + 10]), "+v"(a[o + 11]), "+v"(a[o + 12]), "+v"(a[o + 13]), "+v"(a[o + 14]), "+v"(a[o + 15]) :: "memory")
#define PIN64(a) do { PIN16(a, 0); PIN16(a, 16); PIN16(a, 32); PIN16(a, 48); } while (0)

template <int CTRL, int N> __device__ __forceinline__ void tr_dpp(float (&acc)[64], bool up) {
#pragma unroll
  for (int k = 0; k < N; ++k) {
    const float keep = up ? acc[k + N] : acc[k];
    const float send = up ? acc[k] : acc[k + N];
    acc[k] = keep + dpp<CTRL>(send);
  }
}
template <int NMIN> __device__ __forceinline__ void treduce(float (&acc)[64], int lane) {
#pragma unroll
  for (int k = 0; k < 32; ++k) {
    auto s = __builtin_amdgcn_permlane32_swap(__float_as_uint(acc[k]), __float_as_uint(acc[k + 32]), false, false);
    acc[k] = __uint_as_float(s[0]) + __uint_as_float(s[1]);
  }
#pragma unroll
  for (int k = 0; k < 16; ++k) {
    auto s = __builtin_amdgcn_permlane16_swap(__float_as_uint(acc[k]), __float_as_uint(acc[k + 16]), false, false);
    acc[k] = __uint_as_float(s[0]) + __uint_as_float(s[1]);
  }
  if (NMIN <= 8) tr_dpp<0x128, 8>(acc, (lane & 8) != 0);
}

template <int U> struct SetT { u32x4 r[2 * U]; };
typedef SetT<U> Set;

template <int U> __device__ __forceinline__ void load_kv(SetT<U>& s, const bf16* kp, const bf16* vp) {
#pragma unroll
  for (int u = 0; u < U; ++u) { s.r[u] = ld_nt(kp + u * 4 * DH); s.r[U + u] = ld_nt(vp + u * 4 * DH); }
}

template <int U> __device__ __forceinline__ void compute_tile(const SetT<U>& s, const u32x4& q, float& m, float& l, float (&acc)[8]) {
  constexpr float LOG2E = 1.4426950408889634f;
  float sc[U];
#pragma unroll
  for (int u = 0; u < U; ++u) {
    float d = dot2(s.r[u][0], q[0], 0.f);
    d = dot2(s.r[u][1], q[1], d);
    d = dot2(s.r[u][2], q[2], d);
    d = dot2(s.r[u][3], q[3], d);
    d = row16_sum(d);
    d = rbf(rbf(d) * 0.08838834764831845f) * LOG2E;
    sc[u] = d;
  }
  float tm = sc[0];
#pragma unroll
  for (int u = 1; u < U; ++u) tm = fmaxf(tm, sc[u]);
  const float mn = fmaxf(m, tm);
  const float alpha = __builtin_amdgcn_exp2f(m - mn);
  m = mn;
  float ps = 0.f;
#pragma unroll
  for (int u = 0; u < U; ++u) { sc[u] = __builtin_amdgcn_exp2f(sc[u] - mn); ps += sc[u]; }
  l = fmaf(l, alpha, ps);
#pragma unroll
  for (int k = 0; k < 8; ++k) acc[k] *= alpha;
#pragma unroll
  for (int u = 0; u < U; ++u) {
#pragma unroll
    for (int k = 0; k < 4; ++k) {
      acc[2 * k] = fmaf(sc[u], bf_lo(s.r[U + u][k]), acc[2 * k]);
      acc[2 * k + 1] = fmaf(sc[u], bf_hi(s.r[U + u][k]), acc[2 * k + 1]);
    }
  }
}

__device__ __forceinline__ void layernorm8(const u32x4& xv, float (&xn)[8]) {
  float xf[8];
#pragma unroll
  for (int k = 0; k < 4; ++k) { xf[2 * k] = bf_lo(xv[k]); xf[2 * k + 1] = bf_hi(xv[k]); }
  float s = 0.f;
#pragma unroll
  for (int k = 0; k < 8; ++k) s += xf[k];
  const float mean = wave_sum(s) * (1.f / DM);
  float s2 = 0.f;
#pragma unroll
  for (int k = 0; k < 8; ++k) { const float d = xf[k] - mean; s2 = fmaf(d, d, s2); }
  const float var = wave_sum(s2) * (1.f / DM);
  const float mb = rbf(mean);
  const float den = rbf(sqrtf(rbf(rbf(var) + 1e-5f)));
#pragma unroll
  for (int k = 0; k < 8; ++k) xn[k] = rbf(rbf(xf[k] - mb) / den);
}
__device__ __forceinline__ void layernorm_to_lds(const u32x4& xv, float* lds_xn, int lane) {
  float xn[8];
  layernorm8(xv, xn);
  *reinterpret_cast<f32x4*>(lds_xn + 8 * lane) = f32x4{xn[0], xn[1], xn[2], xn[3]};
  *reinterpret_cast<f32x4*>(lds_xn + 8 * lane + 4) = f32x4{xn[4], xn[5], xn[6], xn[7]};
  __builtin_amdgcn_s_waitcnt(0xc07f);
  __builtin_amdgcn_wave_barrier();
}

typedef __attribute__((ext_vector_type(2))) float f32x2;
__device__ __forceinline__ void o_to_lds(const float* ac, const float* ml, float* o, int lane, bool whole, bool writer, float* ml_out) {
  float M = ml[0];
#pragma unroll
  for (int w = 1; w < NW; ++w) M = fmaxf(M, ml[2 * w]);
  float L = 0.f;
  f32x2 o2 = {0.f, 0.f};
#pragma unroll
  for (int w = 0; w < NW; ++w) {
    const float e = __builtin_amdgcn_exp2f(ml[2 * w] - M);
    L = fmaf(e, ml[2 * w + 1], L);
    o2 += e * *reinterpret_cast<const f32x2*>(ac + w * DH + 2 * lane);
  }
  o2 = f32x2{o2[0] / L, o2[1] / L};
  if (whole) o2 = f32x2{rbf(o2[0]), rbf(o2[1])};
  else if (writer) *reinterpret_cast<f32x2*>(ml_out) = f32x2{M, L};
  *reinterpret_cast<f32x2*>(o + 2 * lane) = o2;
  __builtin_amdgcn_s_waitcnt(0xc07f);
  __builtin_amdgcn_wave_barrier();
}
__device__ __forceinline__ void project(const float* wo_lds, const float* o, int sl, int lane, float* __restrict__ dst) {
  const int rg = lane >> 3;
  const float* wl = wo_lds + (16 * rg) * (DM / 2) + 4 * ((8 * sl + (lane & 7)) ^ (8 * (rg & 1)));
  float y[8];
#pragma unroll
  for (int k = 0; k < 8; ++k) y[k] = 0.f;
#pragma unroll
  for (int i4 = 0; i4 < 4; ++i4) {
    const f32x4 ov = *reinterpret_cast<const f32x4*>(o + 16 * rg + 4 * i4);
#pragma unroll
    for (int i = 0; i < 4; ++i) {
      const u32x4 wv = *reinterpret_cast<const u32x4*>(wl + (4 * i4 + i) * (DM / 2));
#pragma unroll
      for (int k = 0; k < 4; ++k) {
        y[2 * k] = fmaf(ov[i], bf_lo(wv[k]), y[2 * k]);
        y[2 * k + 1] = fmaf(ov[i], bf_hi(wv[k]), y[2 * k + 1]);
      }
    }
  }
#pragma unroll
  for (int k = 0; k < 4; ++k) {
    auto t = __builtin_amdgcn_permlane32_swap(__float_as_uint(y[k]), __float_as_uint(y[k + 4]), false, false);
    y[k] = __uint_as_float(t[0]) + __uint_as_float(t[1]);
  }
#pragma unroll
  for (int k = 0; k < 2; ++k) {
    auto t = __builtin_amdgcn_permlane16_swap(__float_as_uint(y[k]), __float_as_uint(y[k + 2]), false, false);
    y[k] = __uint_as_float(t[0]) + __uint_as_float(t[1]);
  }
  const bool up = (lane & 8) != 0;
  const float yy = (up ? y[1] : y[0]) + dpp<0x128>(up ? y[0] : y[1]);
  dst[64 * sl + 8 * (lane & 7) + 4 * (lane >> 5) + 2 * ((lane >> 4) & 1) + ((lane >> 3) & 1)] = yy;
}

__global__ void __launch_bounds__(NT) attn_ffn_main(const bf16* __restrict__ x, const bf16* __restrict__ kc,
                                                    const bf16* __restrict__ vc, const bf16* __restrict__ wq,
                                                    const bf16* __restrict__ wo, const bf16* __restrict__ wg,
                                                    const bf16* __restrict__ wu, const bf16* __restrict__ wd,
                                                    float* __restrict__ yA, float* __restrict__ yB,
                                                    float* __restrict__ yH, float* __restrict__ mlg) {
  __shared__ __attribute__((aligned(16))) float lds[L_END];
  const int id = blockIdx.x, b = id >> 5, h = id & 31;
  const int tid = threadIdx.x, lane = tid & 63;
  const int wave = __builtin_amdgcn_readfirstlane(tid >> 6);
  const int r = lane >> 4, c = lane & 15;
  const int he = h >> 1, bh = he & 7;
  const bool hostgrp = (h & 1) == 0, host = hostgrp && b == bh, helper = hostgrp && b != bh, dbl = he < NFG - 16;
  STAMP(0);

  if (wave >= NW) {
    const int fw = wave - NW;
    const bool do_ffn = host;
    if (do_ffn) {
#define FFN_PRIO 3
      __builtin_amdgcn_s_setprio(FFN_PRIO);
      const u32x4 xs0 = ld16(x + (2 * fw) * DM + (u32)(8 * lane));
      const u32x4 xs1 = ld16(x + (2 * fw + 1) * DM + (u32)(8 * lane));
      auto gate_off = [&](int j) {
        const int c0 = 64 * j + 8 * (lane & 7);
        return 2u * (u32)((128 * fw + 2 * (lane >> 3)) * FF + (c0 < FF ? c0 : c0 - 32));
      };
      u32 goff = gate_off(he);
      u32x4 w[16];
      auto load_gate = [&]() {
#pragma unroll
        for (int i = 0; i < 16; ++i) w[i] = ld_ffn(wg + (16 * (i >> 1) + (i & 1)) * FF, goff);
      };
      load_gate();
      __builtin_amdgcn_sched_barrier(0);
      {
        float xn0[8], xn1[8];
        layernorm8(xs0, xn0);
        layernorm8(xs1, xn1);
        typedef __attribute__((ext_vector_type(2))) unsigned int u32x2;
#pragma unroll
        for (int k = 0; k < 4; ++k)
          *reinterpret_cast<u32x2*>(lds + L_XA + (4 * lane + k) * NB + 2 * fw) =
              u32x2{pack_bf(xn0[2 * k], xn0[2 * k + 1]), pack_bf(xn1[2 * k], xn1[2 * k + 1])};
        __builtin_amdgcn_s_waitcnt(0xc07f);
      }
      __builtin_amdgcn_sched_barrier(0);
      __builtin_amdgcn_s_barrier();
      STAMP(1);
      float acc[64];
      auto consume = [&](const float* coef, int cstride, bool down, u32 sel_a, u32 sel_b, auto next) {
#pragma unroll
        for (int k = 0; k < 64; ++k) acc[k] = 0.f;
#pragma unroll
        for (int i2 = 0; i2 < 8; ++i2) {
          const u32x4 xa0 = *reinterpret_cast<const u32x4*>(coef + i2 * cstride);
          const u32x4 xa1 = *reinterpret_cast<const u32x4*>(coef + i2 * cstride + 4);
          const u32 sel = i2 < 4 ? sel_a : sel_b, sel2 = sel + (sel == 0x0c0c0c0cu ? 0u : 0x02020202u);
#pragma unroll
          for (int cc = 0; cc < 8; ++cc) {
            const u32 pw = __builtin_amdgcn_perm(w[2 * i2 + 1][cc >> 1], w[2 * i2][cc >> 1], (cc & 1) ? sel2 : sel);
#pragma unroll
            for (int bb = 0; bb < 8; ++bb) {
              const int idx = down ? ((bb >> 2) * 32 + (cc >> 2) * 16 + (bb & 3) * 4 + (cc & 3)) : (bb * 8 + cc);
              acc[idx] = dot2(bb < 4 ? xa0[bb & 3] : xa1[bb & 3], pw, acc[idx]);
            }
            if (cc & 1) __builtin_amdgcn_sched_barrier(0);
          }
          next(2 * i2);
          next(2 * i2 + 1);
          PIN64(acc);
        }
      };
      constexpr u32 SEL = 0x05040100u, SEL0 = 0x0c0c0c0cu;
      const float* xa = lds + L_XA + (64 * fw + (lane >> 3)) * NB;
      volatile LAS unsigned* fl = (volatile LAS unsigned*)(lds + L_FL);
      volatile LAS unsigned* pg = (volatile LAS unsigned*)(lds + L_PG);
      auto ffn_pass = [&](const unsigned pass) __attribute__((always_inline)) {
        const int jj = he + 16 * (int)pass;
        consume(xa, 8 * NB, false, SEL, SEL, [&](int i) { w[i] = ld_ffn(wu + (16 * (i >> 1) + (i & 1)) * FF, goff); });
        treduce<8>(acc, lane);
        if (pass) while (min(min(fl[4], fl[5]), min(fl[6], fl[7])) < pass) __builtin_amdgcn_s_sleep(2);
        asm volatile("" ::: "memory");
        *reinterpret_cast<f32x4*>(lds + L_GP + fw * 512 + 8 * lane) = f32x4{acc[0], acc[1], acc[2], acc[3]};
        *reinterpret_cast<f32x4*>(lds + L_GP + fw * 512 + 8 * lane + 4) = f32x4{acc[4], acc[5], acc[6], acc[7]};
        STAMP(4);
        const bool full = 64 * jj + 64 <= FF;
        const u32 doff = 2u * (u32)(2 * (lane >> 4) * DM + 128 * fw + 8 * (lane & 15));
        const bf16* wd0 = wd + (size_t)(64 * jj) * DM;
        const bf16* wd1 = wd0 + (full ? 32 * DM : 0);
        consume(xa, 8 * NB, false, SEL, SEL, [&](int i) { w[i] = ld_ffn((i < 8 ? wd0 : wd1) + (8 * ((i >> 1) & 3) + (i & 1)) * DM, doff); });
        treduce<8>(acc, lane);
        if (!pass) while ((pg[0] & pg[1] & pg[2] & pg[3] & pg[4] & pg[5] & pg[6] & pg[7]) == 0u) __builtin_amdgcn_s_sleep(2);
        asm volatile("" ::: "memory");
        *reinterpret_cast<f32x4*>(lds + L_UP + fw * 512 + 8 * lane) = f32x4{acc[0], acc[1], acc[2], acc[3]};
        *reinterpret_cast<f32x4*>(lds + L_UP + fw * 512 + 8 * lane + 4) = f32x4{acc[4], acc[5], acc[6], acc[7]};
        STAMP(5);
        __builtin_amdgcn_s_waitcnt(0xc07f);
        if (lane == 0) fl[fw] = pass + 1;
        while (min(min(fl[0], fl[1]), min(fl[2], fl[3])) <= pass) __builtin_amdgcn_s_sleep(2);
        asm volatile("" ::: "memory");
        __builtin_amdgcn_sched_barrier(0);
        STAMP(2);
        {
          f32x4 g0 = {0.f, 0.f, 0.f, 0.f}, g1 = g0, u0 = g0, u1 = g0;
#pragma unroll
          for (int v = 0; v < NFW; ++v) {
            g0 += *reinterpret_cast<const f32x4*>(lds + L_GP + v * 512 + 8 * lane);
            g1 += *reinterpret_cast<const f32x4*>(lds + L_GP + v * 512 + 8 * lane + 4);
            u0 += *reinterpret_cast<const f32x4*>(lds + L_UP + v * 512 + 8 * lane);
            u1 += *reinterpret_cast<const f32x4*>(lds + L_UP + v * 512 + 8 * lane + 4);
          }
          const bool cvalid = 64 * jj + 8 * (lane & 7) < FF;
          float hv[8];
#pragma unroll
          for (int k = 0; k < 8; ++k) {
            const float gg = rbf(k < 4 ? g0[k & 3] : g1[k & 3]), uu = rbf(k < 4 ? u0[k & 3] : u1[k & 3]);
            hv[k] = cvalid ? (gg * (gg > 0.f ? 1.f : 0.f)) * uu : 0.f;
          }
          u32* hb = reinterpret_cast<u32*>(lds + L_HB) + (4 * (lane & 7)) * NB + (lane >> 3);
#pragma unroll
          for (int k = 0; k < 4; ++k) hb[k * NB] = pack_bf(hv[2 * k], hv[2 * k + 1]);
          __builtin_amdgcn_s_waitcnt(0xc07f);
          __builtin_amdgcn_wave_barrier();
          asm volatile("" ::: "memory");
          if (lane == 0) fl[4 + fw] = pass + 1;
          __builtin_amdgcn_sched_barrier(0);
        }
        consume(lds + L_HB + (lane >> 4) * NB, 4 * NB, true, SEL, full ? SEL : SEL0, [&](int) {});
        treduce<16>(acc, lane);
        STAMP(6);
        float* yp = yB + (size_t)jj * (NB * DM) + (size_t)(lane >> 5) * (4 * DM) + 128 * fw + 8 * (lane & 15) + 4 * ((lane >> 4) & 1);
#pragma unroll
        for (int bb = 0; bb < 4; ++bb)
          st_part(yp + bb * DM, f32x4{acc[bb * 4], acc[bb * 4 + 1], acc[bb * 4 + 2], acc[bb * 4 + 3]});
      };
      ffn_pass(0);
      if (dbl) {
        goff = gate_off(he + 16);
        load_gate();
        ffn_pass(1);
      }
    }
    if (helper) {
      __builtin_amdgcn_s_barrier();
      volatile LAS unsigned* pg = (volatile LAS unsigned*)(lds + L_PG);
      while ((pg[0] & pg[1] & pg[2] & pg[3] & pg[4] & pg[5] & pg[6] & pg[7]) == 0u) __builtin_amdgcn_s_sleep(8);
      asm volatile("" ::: "memory");
      o_to_lds(lds + L_AC2, lds + L_ML2, lds + L_O + DH, lane, false, tid == NW * 64, mlg + (he * NB + b) * 2);
      project(lds + L_WO, lds + L_O + DH, 2 * fw, lane, yH + (size_t)(he * NB + b) * DM);
      project(lds + L_WO, lds + L_O + DH, 2 * fw + 1, lane, yH + (size_t)(he * NB + b) * DM);
    }
    STAMP(3);
    return;
  }

  const u32x4 xv = ld16(x + b * DM + 8 * lane);
  const u32x4 xv2 = ld16(x + bh * DM + 8 * lane);
  Set A, Bs;
  u32x4 W[WL];
  const bf16* kp = kc + (size_t)id * (SK * DH) + (size_t)wave * (4 * U * DH) + r * DH + 8 * c;
  const bf16* vp = vc + (size_t)id * (SK * DH) + (size_t)wave * (4 * U * DH) + r * DH + 8 * c;
  constexpr int TSTRIDE = NW * 4 * U * DH;
  {
    const bf16* wqp = wq + (size_t)(wave * 4 + r) * (NH * DH) + h * DH + 8 * c;
#pragma unroll
    for (int i = 0; i < WL; ++i) W[i] = ld_wq(wqp + (size_t)i * (NW * 4) * (NH * DH));
    load_kv(Bs, kp, vp);
    load_kv(A, kp + TSTRIDE, vp + TSTRIDE);
    if (tid < 2 * NFW + NW) lds[L_FL + tid] = 0.f;
  }

  layernorm_to_lds(xv, lds + L_XN, lane);
  if (helper) layernorm_to_lds(xv2, lds + L_XN2, lane);
  STAMP(1);
  {
    const float* xn2 = lds + (helper ? L_XN2 : L_XN);
    float qa[8], qb[8];
#pragma unroll
    for (int k = 0; k < 8; ++k) qa[k] = qb[k] = 0.f;
#pragma unroll
    for (int i = 0; i < WL; ++i) {
      const float xr = lds[L_XN + wave * 4 + r + i * (NW * 4)], xr2 = xn2[wave * 4 + r + i * (NW * 4)];
#pragma unroll
      for (int k = 0; k < 4; ++k) {
        const float w0 = bf_lo(W[i][k]), w1 = bf_hi(W[i][k]);
        qa[2 * k] = fmaf(xr, w0, qa[2 * k]);
        qa[2 * k + 1] = fmaf(xr, w1, qa[2 * k + 1]);
        qb[2 * k] = fmaf(xr2, w0, qb[2 * k]);
        qb[2 * k + 1] = fmaf(xr2, w1, qb[2 * k + 1]);
      }
    }
#pragma unroll
    for (int k = 0; k < 8; ++k) {
      auto s16 = __builtin_amdgcn_permlane16_swap(__float_as_uint(qa[k]), __float_as_uint(qb[k]), false, false);
      const float t = __uint_as_float(s16[0]) + __uint_as_float(s16[1]);
      auto s32 = __builtin_amdgcn_permlane32_swap(__float_as_uint(t), __float_as_uint(t), false, false);
      qa[k] = __uint_as_float(s32[0]) + __uint_as_float(s32[1]);
    }
    if (r == 0 || (r == 1 && helper)) {
      float* qp = lds + (r == 0 ? L_QP : L_QP2) + wave * DH + 8 * c;
      *reinterpret_cast<f32x4*>(qp) = f32x4{qa[0], qa[1], qa[2], qa[3]};
      *reinterpret_cast<f32x4*>(qp + 4) = f32x4{qa[4], qa[5], qa[6], qa[7]};
    }
  }
  auto q_final = [&](const float* qp) {
    f32x4 s0 = {0.f, 0.f, 0.f, 0.f}, s1 = {0.f, 0.f, 0.f, 0.f};
#pragma unroll
    for (int w = 0; w < NW; ++w) {
      s0 += *reinterpret_cast<const f32x4*>(qp + w * DH + 8 * c);
      s1 += *reinterpret_cast<const f32x4*>(qp + w * DH + 8 * c + 4);
    }
    return u32x4{pack_bf(s0[0], s0[1]), pack_bf(s0[2], s0[3]), pack_bf(s1[0], s1[1]), pack_bf(s1[2], s1[3])};
  };
  __syncthreads();
  u32x4 q = q_final(lds + L_QP);
  asm volatile("" : "+v"(q));
  if (host) {
    asm volatile("s_waitcnt lgkmcnt(0)" ::: "memory");
    if (lane == 0) ((volatile LAS unsigned*)(lds + L_PG))[wave] = 1u;
  }

  STAMP(2);
  {
  const bf16* wop = wo + (size_t)(h * DH + wave * WL) * DM + (u32)(8 * (lane ^ (8 * (wave & 1))));
  const unsigned wo_lds = (unsigned)(size_t)(LAS float*)(lds + L_WO) + (unsigned)(wave * WL * DM * 2);
#pragma unroll
  for (int i = 0; i < WL; ++i) glds16_nt(wop + (size_t)i * DM, wo_lds + i * (DM * 2));
  }
  SetT<UF> F, F1;
  if (hostgrp) {
    const int xk = dbl ? 2 * XK : XK;
    const size_t fo = (size_t)(bh * NH + h) * (SK * DH) + (size_t)(SK - NB * xk + xk * b + (xk / NW) * wave + r) * DH + 8 * c;
    load_kv(F, kc + fo, vc + fo);
    if (dbl) load_kv(F1, kc + fo + 4 * UF * DH, vc + fo + 4 * UF * DH);
  }
  auto wave_state_to_lds = [&](float m, float l, float (&acc)[8], float* ac, float* ml) {
    const float mw = xrow_max(m);
    const float sc = __builtin_amdgcn_exp2f(m - mw);
    l = xrow_sum(l * sc);
#pragma unroll
    for (int k = 0; k < 8; ++k) acc[k] = xrow_sum(acc[k] * sc);
    if (r == 0) {
      *reinterpret_cast<f32x4*>(ac + wave * DH + 8 * c) = f32x4{acc[0], acc[1], acc[2], acc[3]};
      *reinterpret_cast<f32x4*>(ac + wave * DH + 8 * c + 4) = f32x4{acc[4], acc[5], acc[6], acc[7]};
      if (c == 0) { ml[2 * wave] = mw; ml[2 * wave + 1] = l; }
    }
  };
  float m = -INFINITY, l = 0.f, acc[8];
#pragma unroll
  for (int k = 0; k < 8; ++k) acc[k] = 0.f;
  compute_tile(Bs, q, m, l, acc);
  load_kv(Bs, kp + (size_t)2 * TSTRIDE, vp + (size_t)2 * TSTRIDE);
  compute_tile(A, q, m, l, acc);
  load_kv(A, kp + (size_t)3 * TSTRIDE, vp + (size_t)3 * TSTRIDE);
  if (host) {
    compute_tile(F, q, m, l, acc);
    if (dbl) compute_tile(F1, q, m, l, acc);
  } else if (helper) {
    const u32x4 q2 = q_final(lds + L_QP2);
    float m2 = -INFINITY, l2 = 0.f, acc2[8];
#pragma unroll
    for (int k = 0; k < 8; ++k) acc2[k] = 0.f;
    compute_tile(F, q2, m2, l2, acc2);
    if (dbl) compute_tile(F1, q2, m2, l2, acc2);
    wave_state_to_lds(m2, l2, acc2, lds + L_AC2, lds + L_ML2);
    __builtin_amdgcn_s_waitcnt(0xc07f);
    if (lane == 0) ((volatile LAS unsigned*)(lds + L_PG))[wave] = 1u;
  }
  const int tend = TILES - 2 - (host ? (dbl ? 2 * XT : XT) : 0);
#pragma unroll 1
  for (int t = 2; t < tend; t += 2) {
    compute_tile(Bs, q, m, l, acc);
    load_kv(Bs, kp + (size_t)(t + 2) * TSTRIDE, vp + (size_t)(t + 2) * TSTRIDE);
    compute_tile(A, q, m, l, acc);
    load_kv(A, kp + (size_t)(t + 3) * TSTRIDE, vp + (size_t)(t + 3) * TSTRIDE);
  }
  compute_tile(Bs, q, m, l, acc);
  compute_tile(A, q, m, l, acc);

  asm volatile("s_waitcnt vmcnt(0)" ::: "memory");
  STAMP(3);
  wave_state_to_lds(m, l, acc, lds + L_AC, lds + L_ML);
  __syncthreads();
  o_to_lds(lds + L_AC, lds + L_ML, lds + L_O, lane, !host, tid == 0, mlg + (he * NB + b) * 2);
  project(lds + L_WO, lds + L_O, wave, lane, yA + (size_t)id * DM);
  STAMP(4);
}

__device__ __forceinline__ float piece_weight(const float* __restrict__ mlg, int hh, int p) {
  f32x4 v[4];
#pragma unroll
  for (int i = 0; i < 4; ++i) v[i] = *reinterpret_cast<const f32x4*>(mlg + hh * (NB * 2) + 4 * i);
  float M = v[0][0];
#pragma unroll
  for (int i = 0; i < 4; ++i) M = fmaxf(M, fmaxf(v[i][0], v[i][2]));
  float L = 0.f, own = 0.f;
#pragma unroll
  for (int i = 0; i < 4; ++i) {
    const float e0 = __builtin_amdgcn_exp2f(v[i][0] - M) * v[i][1], e1 = __builtin_amdgcn_exp2f(v[i][2] - M) * v[i][3];
    L += e0; L += e1;
    own = p == 2 * i ? e0 : (p == 2 * i + 1 ? e1 : own);
  }
  return own / L;
}

__global__ void __launch_bounds__(256) final_sum(const bf16* __restrict__ x, const float* __restrict__ yA,
                                                 const float* __restrict__ yB, const float* __restrict__ yH,
                                                 const float* __restrict__ mlg, bf16* __restrict__ out) {
  __shared__ __attribute__((aligned(16))) float red[2][32][33];
  const int b = blockIdx.x >> 4, cb = blockIdx.x & 15;
  const int tid = threadIdx.x, c4 = tid & 7, g = tid >> 3;
  const int col = cb * 32 + 4 * c4;
  const f32x4 zero = {0.f, 0.f, 0.f, 0.f};
  const int hh = b + 8 * (g >> 3), p = g & 7;
  const bool split = (g & 1) == 0 && ((g >> 1) & 7) == b, piece = g < 16 && p != b;
  f32x4 va = *reinterpret_cast<const f32x4*>(yA + (size_t)(b * NH + g) * DM + col);
  const f32x4 sb = g < NFG ? *reinterpret_cast<const f32x4*>(yB + (size_t)(g * NB + b) * DM + col) : zero;
  const f32x4 vh = piece ? *reinterpret_cast<const f32x4*>(yH + (size_t)(hh * NB + p) * DM + col) : zero;
  if (split) va *= piece_weight(mlg, g >> 1, b);
  if (piece) va += vh * piece_weight(mlg, hh, p);
#pragma unroll
  for (int k = 0; k < 4; ++k) { red[0][g][4 * c4 + k] = va[k]; red[1][g][4 * c4 + k] = sb[k]; }
  __syncthreads();
  if (tid < 32) {
    float a = 0.f, f = 0.f;
#pragma unroll
    for (int i = 0; i < 32; ++i) { a += red[0][i][tid]; f += red[1][i][tid]; }
    const int n = b * DM + cb * 32 + tid;
    const float xa = rbf(__bfloat162float(x[n]) + rbf(a));
    out[n] = __float2bfloat16(xa + rbf(f));
  }
}

extern "C" void kernel_launch(void* const* d_in, const int* in_sizes, int n_in,
                              void* d_out, int out_size, void* d_ws, size_t ws_size,
                              hipStream_t stream) {
  const bf16* x = (const bf16*)d_in[0];
  const bf16* kc = (const bf16*)d_in[1];
  const bf16* vc = (const bf16*)d_in[2];
  const bf16* wq = (const bf16*)d_in[3];
  const bf16* wo = (const bf16*)d_in[4];
  const bf16* wg = (const bf16*)d_in[6];
  const bf16* wu = (const bf16*)d_in[7];
  const bf16* wd = (const bf16*)d_in[8];
  float* yA = (float*)d_ws;
  float* yB = yA + NB * NH * DM;
  float* yH = yB + NFG * NB * DM;
  float* mlg = yH + 16 * NB * DM;
  attn_ffn_main<<<NB * NH, NT, 0, stream>>>(x, kc, vc, wq, wo, wg, wu, wd, yA, yB, yH, mlg);
  final_sum<<<NB * 16, 256, 0, stream>>>(x, yA, yB, yH, mlg, (bf16*)d_out);
}
```

```cpp
#include <hip/hip_runtime.h>
#include <hip/hip_bf16.h>
#include <stdint.h>

typedef unsigned int u32;
typedef __attribute__((ext_vector_type(4))) unsigned int u32x4;
typedef __attribute__((ext_vector_type(4))) float f32x4;
typedef __attribute__((ext_vector_type(2))) __bf16 bf16x2;
typedef __hip_bfloat16 bf16;

constexpr int NB = 8, DM = 512, NH = 32, SK = 4096, DH = 128, FF = 1376;
constexpr int NW = 8;
#define UU 4
constexpr int U = UU;
constexpr int NFW = 4;
constexpr int NT = (NW + NFW) * 64;
constexpr int TILES = SK / (NW * 4 * U);
constexpr int XK = 64;
constexpr int UF = XK / (NW * 4);
constexpr int XT = NB * XK / (NW * 4 * U);
static_assert(TILES >= 4 + 2 * XT && (TILES % 2) == 0 && (XT % 2) == 0 && XT * NW * 4 * U == NB * XK, "tiles");
constexpr int WL = 128 / NW;

#define MIMIC_SCORE_ROUND 1

__device__ __forceinline__ float bf_lo(u32 x) { return __uint_as_float(x << 16); }
__device__ __forceinline__ float bf_hi(u32 x) { return __uint_as_float(x & 0xffff0000u); }
__device__ __forceinline__ float rbf(float x) { return __bfloat162float(__float2bfloat16(x)); }
__device__ __forceinline__ u32 pack_bf(float lo, float hi) {
  return (u32)__bfloat16_as_ushort(__float2bfloat16(lo)) | ((u32)__bfloat16_as_ushort(__float2bfloat16(hi)) << 16);
}
__device__ __forceinline__ float dot2(u32 a, u32 b, float acc) {
  return __builtin_amdgcn_fdot2_f32_bf16(__builtin_bit_cast(bf16x2, a), __builtin_bit_cast(bf16x2, b), acc, false);
}
template <int CTRL> __device__ __forceinline__ float dpp(float x) {
  return __builtin_bit_cast(float, __builtin_amdgcn_mov_dpp(__builtin_bit_cast(int, x), CTRL, 0xf, 0xf, true));
}
__device__ __forceinline__ float row16_sum(float x) {
  x += dpp<0xB1>(x);
  x += dpp<0x4E>(x);
  x += dpp<0x141>(x);
  x += dpp<0x140>(x);
  return x;
}
__device__ __forceinline__ float xrow_sum(float x) {
  auto s = __builtin_amdgcn_permlane16_swap(__float_as_uint(x), __float_as_uint(x), false, false);
  x = __uint_as_float(s[0]) + __uint_as_float(s[1]);
  auto t = __builtin_amdgcn_permlane32_swap(__float_as_uint(x), __float_as_uint(x), false, false);
  return __uint_as_float(t[0]) + __uint_as_float(t[1]);
}
__device__ __forceinline__ float xrow_max(float x) {
  auto s = __builtin_amdgcn_permlane16_swap(__float_as_uint(x), __float_as_uint(x), false, false);
  x = fmaxf(__uint_as_float(s[0]), __uint_as_float(s[1]));
  auto t = __builtin_amdgcn_permlane32_swap(__float_as_uint(x), __float_as_uint(x), false, false);
  return fmaxf(__uint_as_float(t[0]), __uint_as_float(t[1]));
}
__device__ __forceinline__ float wave_sum(float x) { return xrow_sum(row16_sum(x)); }
__device__ __forceinline__ u32x4 ld_nt(const bf16* p) { return __builtin_nontemporal_load(reinterpret_cast<const u32x4*>(p)); }
__device__ __forceinline__ u32x4 ld16(const bf16* p) { return *reinterpret_cast<const u32x4*>(p); }
#define WQ_NT 1
#define WO_NT 1
#define FFN_NT 1
__device__ __forceinline__ u32x4 ld_wq(const bf16* p) { return WQ_NT ? ld_nt(p) : ld16(p); }
__device__ __forceinline__ u32x4 ld_wo(const bf16* p) { return WO_NT ? ld_nt(p) : ld16(p); }
__device__ __forceinline__ u32x4 ld_ffn(const bf16* base, u32 boff) {
  return __builtin_nontemporal_load(reinterpret_cast<const u32x4*>(reinterpret_cast<const char*>(base) + boff));
}

#define ST_MODE 0
__device__ __forceinline__ void st_part(float* p, f32x4 v) {
  *reinterpret_cast<f32x4*>(p) = v;
}
constexpr int L_WO = 0;
constexpr int L_XN = L_WO + DH * DM / 2;
constexpr int L_QP = L_XN + DM;
constexpr int L_UP = L_XN;
constexpr int L_AC = L_UP + NFW * NB * 64;
constexpr int L_ML = L_AC + NW * DH;
constexpr int L_O = L_ML + NW * 2;
constexpr int L_HB = L_O + 2 * DH;
constexpr int L_FL = L_HB + 32 * NB;
constexpr int L_PG = L_FL + 8;
constexpr int L_XA = L_PG + 8;
constexpr int L_GP = L_XA + 256 * NB;
constexpr int L_END = L_GP + NFW * NB * 64;
static_assert(L_QP + NW * DH <= L_AC, "LDS");
constexpr int L_XN2 = L_XA, L_QP2 = L_XN2 + DM, L_AC2 = L_QP2 + NW * DH, L_ML2 = L_AC2 + NW * DH;
static_assert(L_ML2 + NW * 2 <= L_END && L_END * 4 <= 163840 && (L_XA % 4) == 0 && (L_FL % 4) == 0 && (L_O % 4) == 0, "LDS");
constexpr int NFG = (FF + 63) / 64;
#define LAS __attribute__((address_space(3)))

__device__ __forceinline__ void glds16_nt(const void* gsrc, unsigned lds_dst) {
  unsigned keep;
  asm volatile("s_mov_b32 %0, m0\n\ts_mov_b32 m0, %2\n\ts_nop 0\n\tglobal_load_lds_dwordx4 %1, off nt\n\ts_mov_b32 m0, %0"
               : "=&s"(keep) : "v"(gsrc), "s"(lds_dst) : "memory");
}

#define STAMP(i)
#define PIN16(a, o) asm volatile("" : "+v"(a[o]), "+v"(a[o + 1]), "+v"(a[o + 2]), "+v"(a[o + 3]), "+v"(a[o + 4]), "+v"(a[o + 5]), "+v"(a[o + 6]), \
  "+v"(a[o + 7]), "+v"(a[o + 8]), "+v"(a[o + 9]), "+v"(a[o + 10]), "+v"(a[o + 11]), "+v"(a[o + 12]), "+v"(a[o + 13]), "+v"(a[o + 14]), "+v"(a[o + 15]) :: "memory")
#define PIN64(a) do { PIN16(a, 0); PIN16(a, 16); PIN16(a, 32); PIN16(a, 48); } while (0)

template <int CTRL, int N> __device__ __forceinline__ void tr_dpp(float (&acc)[64], bool up) {
#pragma unroll
  for (int k = 0; k < N; ++k) {
    const float keep = up ? acc[k + N] : acc[k];
    const float send = up ? acc[k] : acc[k + N];
    acc[k] = keep + dpp<CTRL>(send);
  }
}
template <int NMIN> __device__ __forceinline__ void treduce(float (&acc)[64], int lane) {
#pragma unroll
  for (int k = 0; k < 32; ++k) {
    auto s = __builtin_amdgcn_permlane32_swap(__float_as_uint(acc[k]), __float_as_uint(acc[k + 32]), false, false);
    acc[k] = __uint_as_float(s[0]) + __uint_as_float(s[1]);
  }
#pragma unroll
  for (int k = 0; k < 16; ++k) {
    auto s = __builtin_amdgcn_permlane16_swap(__float_as_uint(acc[k]), __float_as_uint(acc[k + 16]), false, false);
    acc[k] = __uint_as_float(s[0]) + __uint_as_float(s[1]);
  }
  if (NMIN <= 8) tr_dpp<0x128, 8>(acc, (lane & 8) != 0);
}

template <int U> struct SetT { u32x4 r[2 * U]; };
typedef SetT<U> Set;

template <int U> __device__ __forceinline__ void load_kv(SetT<U>& s, const bf16* kp, const bf16* vp) {
#pragma unroll
  for (int u = 0; u < U; ++u) { s.r[u] = ld_nt(kp + u * 4 * DH); s.r[U + u] = ld_nt(vp + u * 4 * DH); }
}

template <int U> __device__ __forceinline__ void compute_tile(const SetT<U>& s, const u32x4& q, float& m, float& l, float (&acc)[8]) {
  constexpr float LOG2E = 1.4426950408889634f;
  float sc[U];
#pragma unroll
  for (int u = 0; u < U; ++u) {
    float d = dot2(s.r[u][0], q[0], 0.f);
    d = dot2(s.r[u][1], q[1], d);
    d = dot2(s.r[u][2], q[2], d);
    d = dot2(s.r[u][3], q[3], d);
    d = row16_sum(d);
    d = rbf(rbf(d) * 0.08838834764831845f) * LOG2E;
    sc[u] = d;
  }
  float tm = sc[0];
#pragma unroll
  for (int u = 1; u < U; ++u) tm = fmaxf(tm, sc[u]);
  const float mn = fmaxf(m, tm);
  const float alpha = __builtin_amdgcn_exp2f(m - mn);
  m = mn;
  float ps = 0.f;
#pragma unroll
  for (int u = 0; u < U; ++u) { sc[u] = __builtin_amdgcn_exp2f(sc[u] - mn); ps += sc[u]; }
  l = fmaf(l, alpha, ps);
#pragma unroll
  for (int k = 0; k < 8; ++k) acc[k] *= alpha;
#pragma unroll
  for (int u = 0; u < U; ++u) {
#pragma unroll
    for (int k = 0; k < 4; ++k) {
      acc[2 * k] = fmaf(sc[u], bf_lo(s.r[U + u][k]), acc[2 * k]);
      acc[2 * k + 1] = fmaf(sc[u], bf_hi(s.r[U + u][k]), acc[2 * k + 1]);
    }
  }
}

__device__ __forceinline__ void layernorm8(const u32x4& xv, float (&xn)[8]) {
  float xf[8];
#pragma unroll
  for (int k = 0; k < 4; ++k) { xf[2 * k] = bf_lo(xv[k]); xf[2 * k + 1] = bf_hi(xv[k]); }
  float s = 0.f;
#pragma unroll
  for (int k = 0; k < 8; ++k) s += xf[k];
  const float mean = wave_sum(s) * (1.f / DM);
  float s2 = 0.f;
#pragma unroll
  for (int k = 0; k < 8; ++k) { const float d = xf[k] - mean; s2 = fmaf(d, d, s2); }
  const float var = wave_sum(s2) * (1.f / DM);
  const float mb = rbf(mean);
  const float den = rbf(sqrtf(rbf(rbf(var) + 1e-5f)));
#pragma unroll
  for (int k = 0; k < 8; ++k) xn[k] = rbf(rbf(xf[k] - mb) / den);
}
__device__ __forceinline__ void layernorm_to_lds(const u32x4& xv, float* lds_xn, int lane) {
  float xn[8];
  layernorm8(xv, xn);
  *reinterpret_cast<f32x4*>(lds_xn + 8 * lane) = f32x4{xn[0], xn[1], xn[2], xn[3]};
  *reinterpret_cast<f32x4*>(lds_xn + 8 * lane + 4) = f32x4{xn[4], xn[5], xn[6], xn[7]};
  __builtin_amdgcn_s_waitcnt(0xc07f);
  __builtin_amdgcn_wave_barrier();
}

typedef __attribute__((ext_vector_type(2))) float f32x2;
__device__ __forceinline__ void o_to_lds(const float* ac, const float* ml, float* o, int lane, bool whole, bool writer, float* ml_out) {
  float M = ml[0];
#pragma unroll
  for (int w = 1; w < NW; ++w) M = fmaxf(M, ml[2 * w]);
  float L = 0.f;
  f32x2 o2 = {0.f, 0.f};
#pragma unroll
  for (int w = 0; w < NW; ++w) {
    const float e = __builtin_amdgcn_exp2f(ml[2 * w] - M);
    L = fmaf(e, ml[2 * w + 1], L);
    o2 += e * *reinterpret_cast<const f32x2*>(ac + w * DH + 2 * lane);
  }
  o2 = f32x2{o2[0] / L, o2[1] / L};
  if (whole) o2 = f32x2{rbf(o2[0]), rbf(o2[1])};
  else if (writer) *reinterpret_cast<f32x2*>(ml_out) = f32x2{M, L};
  *reinterpret_cast<f32x2*>(o + 2 * lane) = o2;
  __builtin_amdgcn_s_waitcnt(0xc07f);
  __builtin_amdgcn_wave_barrier();
}
__device__ __forceinline__ void project(const float* wo_lds, const float* o, int sl, int lane, float* __restrict__ dst) {
  const int rg = lane >> 3;
  const float* wl = wo_lds + (16 * rg) * (DM / 2) + 4 * ((8 * sl + (lane & 7)) ^ (8 * (rg & 1)));
  float y[8];
#pragma unroll
  for (int k = 0; k < 8; ++k) y[k] = 0.f;
#pragma unroll
  for (int i4 = 0; i4 < 4; ++i4) {
    const f32x4 ov = *reinterpret_cast<const f32x4*>(o + 16 * rg + 4 * i4);
#pragma unroll
    for (int i = 0; i < 4; ++i) {
      const u32x4 wv = *reinterpret_cast<const u32x4*>(wl + (4 * i4 + i) * (DM / 2));
#pragma unroll
      for (int k = 0; k < 4; ++k) {
        y[2 * k] = fmaf(ov[i], bf_lo(wv[k]), y[2 * k]);
        y[2 * k + 1] = fmaf(ov[i], bf_hi(wv[k]), y[2 * k + 1]);
      }
    }
  }
#pragma unroll
  for (int k = 0; k < 4; ++k) {
    auto t = __builtin_amdgcn_permlane32_swap(__float_as_uint(y[k]), __float_as_uint(y[k + 4]), false, false);
    y[k] = __uint_as_float(t[0]) + __uint_as_float(t[1]);
  }
#pragma unroll
  for (int k = 0; k < 2; ++k) {
    auto t = __builtin_amdgcn_permlane16_swap(__float_as_uint(y[k]), __float_as_uint(y[k + 2]), false, false);
    y[k] = __uint_as_float(t[0]) + __uint_as_float(t[1]);
  }
  const bool up = (lane & 8) != 0;
  const float yy = (up ? y[1] : y[0]) + dpp<0x128>(up ? y[0] : y[1]);
  dst[64 * sl + 8 * (lane & 7) + 4 * (lane >> 5) + 2 * ((lane >> 4) & 1) + ((lane >> 3) & 1)] = yy;
}

__global__ void __launch_bounds__(NT) attn_ffn_main(const bf16* __restrict__ x, const bf16* __restrict__ kc,
                                                    const bf16* __restrict__ vc, const bf16* __restrict__ wq,
                                                    const bf16* __restrict__ wo, const bf16* __restrict__ wg,
                                                    const bf16* __restrict__ wu, const bf16* __restrict__ wd,
                                                    float* __restrict__ yA, float* __restrict__ yB,
                                                    float* __restrict__ yH, float* __restrict__ mlg) {
  __shared__ __attribute__((aligned(16))) float lds[L_END];
  const int id = blockIdx.x, b = id >> 5, h = id & 31;
  const int tid = threadIdx.x, lane = tid & 63;
  const int wave = __builtin_amdgcn_readfirstlane(tid >> 6);
  const int r = lane >> 4, c = lane & 15;
  const int he = h >> 1, bh = he & 7;
  const bool hostgrp = (h & 1) == 0, host = hostgrp && b == bh, helper = hostgrp && b != bh, dbl = he < NFG - 16;
  STAMP(0);

  if (wave >= NW) {
    const int fw = wave - NW;
    const bool do_ffn = host;
    if (do_ffn) {
#define FFN_PRIO 3
      __builtin_amdgcn_s_setprio(FFN_PRIO);
      const u32x4 xs0 = ld16(x + (2 * fw) * DM + (u32)(8 * lane));
      const u32x4 xs1 = ld16(x + (2 * fw + 1) * DM + (u32)(8 * lane));
      auto gate_off = [&](int j) {
        const int c0 = 64 * j + 8 * (lane & 7);
        return 2u * (u32)((128 * fw + 2 * (lane >> 3)) * FF + (c0 < FF ? c0 : c0 - 32));
      };
      u32 goff = gate_off(he);
      u32x4 w[16];
      auto load_gate = [&]() {
#pragma unroll
        for (int i = 0; i < 16; ++i) w[i] = ld_ffn(wg + (16 * (i >> 1) + (i & 1)) * FF, goff);
      };
      load_gate();
      __builtin_amdgcn_sched_barrier(0);
      {
        float xn0[8], xn1[8];
        layernorm8(xs0, xn0);
        layernorm8(xs1, xn1);
        typedef __attribute__((ext_vector_type(2))) unsigned int u32x2;
#pragma unroll
        for (int k = 0; k < 4; ++k)
          *reinterpret_cast<u32x2*>(lds + L_XA + (4 * lane + k) * NB + 2 * fw) =
              u32x2{pack_bf(xn0[2 * k], xn0[2 * k + 1]), pack_bf(xn1[2 * k], xn1[2 * k + 1])};
        __builtin_amdgcn_s_waitcnt(0xc07f);
      }
      __builtin_amdgcn_sched_barrier(0);
      __builtin_amdgcn_s_barrier();
      STAMP(1);
      float acc[64];
      auto consume = [&](const float* coef, int cstride, bool down, u32 sel_a, u32 sel_b, auto next) {
#pragma unroll
        for (int k = 0; k < 64; ++k) acc[k] = 0.f;
#pragma unroll
        for (int i2 = 0; i2 < 8; ++i2) {
          const u32x4 xa0 = *reinterpret_cast<const u32x4*>(coef + i2 * cstride);
          const u32x4 xa1 = *reinterpret_cast<const u32x4*>(coef + i2 * cstride + 4);
          const u32 sel = i2 < 4 ? sel_a : sel_b, sel2 = sel + (sel == 0x0c0c0c0cu ? 0u : 0x02020202u);
#pragma unroll
          for (int cc = 0; cc < 8; ++cc) {
            const u32 pw = __builtin_amdgcn_perm(w[2 * i2 + 1][cc >> 1], w[2 * i2][cc >> 1], (cc & 1) ? sel2 : sel);
#pragma unroll
            for (int bb = 0; bb < 8; ++bb) {
              const int idx = down ? ((bb >> 2) * 32 + (cc >> 2) * 16 + (bb & 3) * 4 + (cc & 3)) : (bb * 8 + cc);
              acc[idx] = dot2(bb < 4 ? xa0[bb & 3] : xa1[bb & 3], pw, acc[idx]);
            }
            if (cc & 1) __builtin_amdgcn_sched_barrier(0);
          }
          next(2 * i2);
          next(2 * i2 + 1);
          PIN64(acc);
        }
      };
      constexpr u32 SEL = 0x05040100u, SEL0 = 0x0c0c0c0cu;
      const float* xa = lds + L_XA + (64 * fw + (lane >> 3)) * NB;
      volatile LAS unsigned* fl = (volatile LAS unsigned*)(lds + L_FL);
      volatile LAS unsigned* pg = (volatile LAS unsigned*)(lds + L_PG);
      auto ffn_pass = [&](const unsigned pass) __attribute__((always_inline)) {
        const int jj = he + 16 * (int)pass;
        consume(xa, 8 * NB, false, SEL, SEL, [&](int i) { w[i] = ld_ffn(wu + (16 * (i >> 1) + (i & 1)) * FF, goff); });
        treduce<8>(acc, lane);
        if (pass) while (min(min(fl[4], fl[5]), min(fl[6], fl[7])) < pass) __builtin_amdgcn_s_sleep(2);
        asm volatile("" ::: "memory");
        *reinterpret_cast<f32x4*>(lds + L_GP + fw * 512 + 8 * lane) = f32x4{acc[0], acc[1], acc[2], acc[3]};
        *reinterpret_cast<f32x4*>(lds + L_GP + fw * 512 + 8 * lane + 4) = f32x4{acc[4], acc[5], acc[6], acc[7]};
        STAMP(4);
        const bool full = 64 * jj + 64 <= FF;
        const u32 doff = 2u * (u32)(2 * (lane >> 4) * DM + 128 * fw + 8 * (lane & 15));
        const bf16* wd0 = wd + (size_t)(64 * jj) * DM;
        const bf16* wd1 = wd0 + (full ? 32 * DM : 0);
        consume(xa, 8 * NB, false, SEL, SEL, [&](int i) { w[i] = ld_ffn((i < 8 ? wd0 : wd1) + (8 * ((i >> 1) & 3) + (i & 1)) * DM, doff); });
        treduce<8>(acc, lane);
        if (!pass) while ((pg[0] & pg[1] & pg[2] & pg[3] & pg[4] & pg[5] & pg[6] & pg[7]) == 0u) __builtin_amdgcn_s_sleep(2);
        asm volatile("" ::: "memory");
        *reinterpret_cast<f32x4*>(lds + L_UP + fw * 512 + 8 * lane) = f32x4{acc[0], acc[1], acc[2], acc[3]};
        *reinterpret_cast<f32x4*>(lds + L_UP + fw * 512 + 8 * lane + 4) = f32x4{acc[4], acc[5], acc[6], acc[7]};
        STAMP(5);
        __builtin_amdgcn_s_waitcnt(0xc07f);
        if (lane == 0) fl[fw] = pass + 1;
        while (min(min(fl[0], fl[1]), min(fl[2], fl[3])) <= pass) __builtin_amdgcn_s_sleep(2);
        asm volatile("" ::: "memory");
        __builtin_amdgcn_sched_barrier(0);
        STAMP(2);
        {
          f32x4 g0 = {0.f, 0.f, 0.f, 0.f}, g1 = g0, u0 = g0, u1 = g0;
#pragma unroll
          for (int v = 0; v < NFW; ++v) {
            g0 += *reinterpret_cast<const f32x4*>(lds + L_GP + v * 512 + 8 * lane);
            g1 += *reinterpret_cast<const f32x4*>(lds + L_GP + v * 512 + 8 * lane + 4);
            u0 += *reinterpret_cast<const f32x4*>(lds + L_UP + v * 512 + 8 * lane);
            u1 += *reinterpret_cast<const f32x4*>(lds + L_UP + v * 512 + 8 * lane + 4);
          }
          const bool cvalid = 64 * jj + 8 * (lane & 7) < FF;
          float hv[8];
#pragma unroll
          for (int k = 0; k < 8; ++k) {
            const float gg = rbf(k < 4 ? g0[k & 3] : g1[k & 3]), uu = rbf(k < 4 ? u0[k & 3] : u1[k & 3]);
            hv[k] = cvalid ? (gg * (gg > 0.f ? 1.f : 0.f)) * uu : 0.f;
          }
          u32* hb = reinterpret_cast<u32*>(lds + L_HB) + (4 * (lane & 7)) * NB + (lane >> 3);
#pragma unroll
          for (int k = 0; k < 4; ++k) hb[k * NB] = pack_bf(hv[2 * k], hv[2 * k + 1]);
          __builtin_amdgcn_s_waitcnt(0xc07f);
          __builtin_amdgcn_wave_barrier();
          asm volatile("" ::: "memory");
          if (lane == 0) fl[4 + fw] = pass + 1;
          __builtin_amdgcn_sched_barrier(0);
        }
        consume(lds + L_HB + (lane >> 4) * NB, 4 * NB, true, SEL, full ? SEL : SEL0, [&](int) {});
        treduce<16>(acc, lane);
        STAMP(6);
        float* yp = yB + (size_t)jj * (NB * DM) + (size_t)(lane >> 5) * (4 * DM) + 128 * fw + 8 * (lane & 15) + 4 * ((lane >> 4) & 1);
#pragma unroll
        for (int bb = 0; bb < 4; ++bb)
          st_part(yp + bb * DM, f32x4{acc[bb * 4], acc[bb * 4 + 1], acc[bb * 4 + 2], acc[bb * 4 + 3]});
      };
      ffn_pass(0);
      if (dbl) {
        goff = gate_off(he + 16);
        load_gate();
        ffn_pass(1);
      }
    }
    if (helper) {
      __builtin_amdgcn_s_barrier();
      volatile LAS unsigned* pg = (volatile LAS unsigned*)(lds + L_PG);
      while ((pg[0] & pg[1] & pg[2] & pg[3] & pg[4] & pg[5] & pg[6] & pg[7]) == 0u) __builtin_amdgcn_s_sleep(8);
      asm volatile("" ::: "memory");
      o_to_lds(lds + L_AC2, lds + L_ML2, lds + L_O + DH, lane, false, tid == NW * 64, mlg + (he * NB + b) * 2);
      project(lds + L_WO, lds + L_O + DH, 2 * fw, lane, yH + (size_t)(he * NB + b) * DM);
      project(lds + L_WO, lds + L_O + DH, 2 * fw + 1, lane, yH + (size_t)(he * NB + b) * DM);
    }
    STAMP(3);
    return;
  }

  const u32x4 xv = ld16(x + b * DM + 8 * lane);
  const u32x4 xv2 = ld16(x + bh * DM + 8 * lane);
  Set A, Bs;
  u32x4 W[WL];
  const bf16* kp = kc + (size_t)id * (SK * DH) + (size_t)wave * (4 * U * DH) + r * DH + 8 * c;
  const bf16* vp = vc + (size_t)id * (SK * DH) + (size_t)wave * (4 * U * DH) + r * DH + 8 * c;
  constexpr int TSTRIDE = NW * 4 * U * DH;
  {
    const bf16* wqp = wq + (size_t)(wave * 4 + r) * (NH * DH) + h * DH + 8 * c;
#define ORDER 1
#define LDW(i) W[i] = ld_wq(wqp + (size_t)(i) * (NW * 4) * (NH * DH))
#pragma unroll
    for (int u = 0; u < U; ++u) {
      Bs.r[u] = ld_nt(kp + u * 4 * DH);
      LDW(4 * u); LDW(4 * u + 1);
      Bs.r[U + u] = ld_nt(vp + u * 4 * DH);
      LDW(4 * u + 2); LDW(4 * u + 3);
      __builtin_amdgcn_sched_barrier(0);
    }
    load_kv(A, kp + TSTRIDE, vp + TSTRIDE);
    __builtin_amdgcn_sched_barrier(0);
    if (tid < 2 * NFW + NW) lds[L_FL + tid] = 0.f;
  }

  layernorm_to_lds(xv, lds + L_XN, lane);
  if (helper) layernorm_to_lds(xv2, lds + L_XN2, lane);
  STAMP(1);
  {
    const float* xn2 = lds + (helper ? L_XN2 : L_XN);
    float qa[8], qb[8];
#pragma unroll
    for (int k = 0; k < 8; ++k) qa[k] = qb[k] = 0.f;
#pragma unroll
    for (int i = 0; i < WL; ++i) {
      const float xr = lds[L_XN + wave * 4 + r + i * (NW * 4)], xr2 = xn2[wave * 4 + r + i * (NW * 4)];
#pragma unroll
      for (int k = 0; k < 4; ++k) {
        const float w0 = bf_lo(W[i][k]), w1 = bf_hi(W[i][k]);
        qa[2 * k] = fmaf(xr, w0, qa[2 * k]);
        qa[2 * k + 1] = fmaf(xr, w1, qa[2 * k + 1]);
        qb[2 * k] = fmaf(xr2, w0, qb[2 * k]);
        qb[2 * k + 1] = fmaf(xr2, w1, qb[2 * k + 1]);
      }
    }
#pragma unroll
    for (int k = 0; k < 8; ++k) {
      auto s16 = __builtin_amdgcn_permlane16_swap(__float_as_uint(qa[k]), __float_as_uint(qb[k]), false, false);
      const float t = __uint_as_float(s16[0]) + __uint_as_float(s16[1]);
      auto s32 = __builtin_amdgcn_permlane32_swap(__float_as_uint(t), __float_as_uint(t), false, false);
      qa[k] = __uint_as_float(s32[0]) + __uint_as_float(s32[1]);
    }
    if (r == 0 || (r == 1 && helper)) {
      float* qp = lds + (r == 0 ? L_QP : L_QP2) + wave * DH + 8 * c;
      *reinterpret_cast<f32x4*>(qp) = f32x4{qa[0], qa[1], qa[2], qa[3]};
      *reinterpret_cast<f32x4*>(qp + 4) = f32x4{qa[4], qa[5], qa[6], qa[7]};
    }
  }
  auto q_final = [&](const float* qp) {
    f32x4 s0 = {0.f, 0.f, 0.f, 0.f}, s1 = {0.f, 0.f, 0.f, 0.f};
#pragma unroll
    for (int w = 0; w < NW; ++w) {
      s0 += *reinterpret_cast<const f32x4*>(qp + w * DH + 8 * c);
      s1 += *reinterpret_cast<const f32x4*>(qp + w * DH + 8 * c + 4);
    }
    return u32x4{pack_bf(s0[0], s0[1]), pack_bf(s0[2], s0[3]), pack_bf(s1[0], s1[1]), pack_bf(s1[2], s1[3])};
  };
  __syncthreads();
  u32x4 q = q_final(lds + L_QP);
  asm volatile("" : "+v"(q));
  if (host) {
    asm volatile("s_waitcnt lgkmcnt(0)" ::: "memory");
    if (lane == 0) ((volatile LAS unsigned*)(lds + L_PG))[wave] = 1u;
  }

  STAMP(2);
  {
  const bf16* wop = wo + (size_t)(h * DH + wave * WL) * DM + (u32)(8 * (lane ^ (8 * (wave & 1))));
  const unsigned wo_lds = (unsigned)(size_t)(LAS float*)(lds + L_WO) + (unsigned)(wave * WL * DM * 2);
#pragma unroll
  for (int i = 0; i < WL; ++i) glds16_nt(wop + (size_t)i * DM, wo_lds + i * (DM * 2));
  }
  SetT<UF> F, F1;
  if (hostgrp) {
    const int xk = dbl ? 2 * XK : XK;
    const size_t fo = (size_t)(bh * NH + h) * (SK * DH) + (size_t)(SK - NB * xk + xk * b + (xk / NW) * wave + r) * DH + 8 * c;
    load_kv(F, kc + fo, vc + fo);
    if (dbl) load_kv(F1, kc + fo + 4 * UF * DH, vc + fo + 4 * UF * DH);
  }
  auto wave_state_to_lds = [&](float m, float l, float (&acc)[8], float* ac, float* ml) {
    const float mw = xrow_max(m);
    const float sc = __builtin_amdgcn_exp2f(m - mw);
    l = xrow_sum(l * sc);
#pragma unroll
    for (int k = 0; k < 8; ++k) acc[k] = xrow_sum(acc[k] * sc);
    if (r == 0) {
      *reinterpret_cast<f32x4*>(ac + wave * DH + 8 * c) = f32x4{acc[0], acc[1], acc[2], acc[3]};
      *reinterpret_cast<f32x4*>(ac + wave * DH + 8 * c + 4) = f32x4{acc[4], acc[5], acc[6], acc[7]};
      if (c == 0) { ml[2 * wave] = mw; ml[2 * wave + 1] = l; }
    }
  };
  float m = -INFINITY, l = 0.f, acc[8];
#pragma unroll
  for (int k = 0; k < 8; ++k) acc[k] = 0.f;
  compute_tile(Bs, q, m, l, acc);
  load_kv(Bs, kp + (size_t)2 * TSTRIDE, vp + (size_t)2 * TSTRIDE);
  compute_tile(A, q, m, l, acc);
  load_kv(A, kp + (size_t)3 * TSTRIDE, vp + (size_t)3 * TSTRIDE);
  if (host) {
    compute_tile(F, q, m, l, acc);
    if (dbl) compute_tile(F1, q, m, l, acc);
  } else if (helper) {
    const u32x4 q2 = q_final(lds + L_QP2);
    float m2 = -INFINITY, l2 = 0.f, acc2[8];
#pragma unroll
    for (int k = 0; k < 8; ++k) acc2[k] = 0.f;
    compute_tile(F, q2, m2, l2, acc2);
    if (dbl) compute_tile(F1, q2, m2, l2, acc2);
    wave_state_to_lds(m2, l2, acc2, lds + L_AC2, lds + L_ML2);
    __builtin_amdgcn_s_waitcnt(0xc07f);
    if (lane == 0) ((volatile LAS unsigned*)(lds + L_PG))[wave] = 1u;
  }
  const int tend = TILES - 2 - (host ? (dbl ? 2 * XT : XT) : 0);
#pragma unroll 1
  for (int t = 2; t < tend; t += 2) {
    compute_tile(Bs, q, m, l, acc);
    load_kv(Bs, kp + (size_t)(t + 2) * TSTRIDE, vp + (size_t)(t + 2) * TSTRIDE);
    compute_tile(A, q, m, l, acc);
    load_kv(A, kp + (size_t)(t + 3) * TSTRIDE, vp + (size_t)(t + 3) * TSTRIDE);
  }
  compute_tile(Bs, q, m, l, acc);
  compute_tile(A, q, m, l, acc);

  asm volatile("s_waitcnt vmcnt(0)" ::: "memory");
  STAMP(3);
  wave_state_to_lds(m, l, acc, lds + L_AC, lds + L_ML);
  __syncthreads();
  o_to_lds(lds + L_AC, lds + L_ML, lds + L_O, lane, !host, tid == 0, mlg + (he * NB + b) * 2);
  project(lds + L_WO, lds + L_O, wave, lane, yA + (size_t)id * DM);
  STAMP(4);
}

__device__ __forceinline__ float piece_weight(const float* __restrict__ mlg, int hh, int p) {
  f32x4 v[4];
#pragma unroll
  for (int i = 0; i < 4; ++i) v[i] = *reinterpret_cast<const f32x4*>(mlg + hh * (NB * 2) + 4 * i);
  float M = v[0][0];
#pragma unroll
  for (int i = 0; i < 4; ++i) M = fmaxf(M, fmaxf(v[i][0], v[i][2]));
  float L = 0.f, own = 0.f;
#pragma unroll
  for (int i = 0; i < 4; ++i) {
    const float e0 = __builtin_amdgcn_exp2f(v[i][0] - M) * v[i][1], e1 = __builtin_amdgcn_exp2f(v[i][2] - M) * v[i][3];
    L += e0; L += e1;
    own = p == 2 * i ? e0 : (p == 2 * i + 1 ? e1 : own);
  }
  return own / L;
}

__global__ void __launch_bounds__(256) final_sum(const bf16* __restrict__ x, const float* __restrict__ yA,
                                                 const float* __restrict__ yB, const float* __restrict__ yH,
                                                 const float* __restrict__ mlg, bf16* __restrict__ out) {
  __shared__ __attribute__((aligned(16))) float red[2][4][32];
  const int b = blockIdx.x >> 4, cb = blockIdx.x & 15;
  const int tid = threadIdx.x, c4 = tid & 7, g = tid >> 3;
  const int col = cb * 32 + 4 * c4;
  const f32x4 zero = {0.f, 0.f, 0.f, 0.f};
  const int hh = b + 8 * (g >> 3), p = g & 7;
  const bool split = (g & 1) == 0 && ((g >> 1) & 7) == b, piece = g < 16 && p != b;
  const int n = b * DM + cb * 32 + (tid & 31);
  const float xv = __bfloat162float(x[n]);
  f32x4 va = *reinterpret_cast<const f32x4*>(yA + (size_t)(b * NH + g) * DM + col);
  f32x4 sb = g < NFG ? *reinterpret_cast<const f32x4*>(yB + (size_t)(g * NB + b) * DM + col) : zero;
  const f32x4 vh = piece ? *reinterpret_cast<const f32x4*>(yH + (size_t)(hh * NB + p) * DM + col) : zero;
  if (split) va *= piece_weight(mlg, g >> 1, b);
  if (piece) va += vh * piece_weight(mlg, hh, p);
#pragma unroll
  for (int k = 0; k < 4; ++k) {
    va[k] = xrow_sum(va[k] + dpp<0x128>(va[k]));
    sb[k] = xrow_sum(sb[k] + dpp<0x128>(sb[k]));
  }
  if ((tid & 63) < 8) {
    *reinterpret_cast<f32x4*>(&red[0][tid >> 6][4 * c4]) = va;
    *reinterpret_cast<f32x4*>(&red[1][tid >> 6][4 * c4]) = sb;
  }
  __syncthreads();
  if (tid < 32) {
    const float a = (red[0][0][tid] + red[0][1][tid]) + (red[0][2][tid] + red[0][3][tid]);
    const float f = (red[1][0][tid] + red[1][1][tid]) + (red[1][2][tid] + red[1][3][tid]);
    out[n] = __float2bfloat16(rbf(xv + rbf(a)) + rbf(f));
  }
}

extern "C" void kernel_launch(void* const* d_in, const int* in_sizes, int n_in,
                              void* d_out, int out_size, void* d_ws, size_t ws_size,
                              hipStream_t stream) {
  const bf16* x = (const bf16*)d_in[0];
  const bf16* kc = (const bf16*)d_in[1];
  const bf16* vc = (const bf16*)d_in[2];
  const bf16* wq = (const bf16*)d_in[3];
  const bf16* wo = (const bf16*)d_in[4];
  const bf16* wg = (const bf16*)d_in[6];
  const bf16* wu = (const bf16*)d_in[7];
  const bf16* wd = (const bf16*)d_in[8];
  float* yA = (float*)d_ws;
  float* yB = yA + NB * NH * DM;
  float* yH = yB + NFG * NB * DM;
  float* mlg = yH + 16 * NB * DM;
  attn_ffn_main<<<NB * NH, NT, 0, stream>>>(x, kc, vc, wq, wo, wg, wu, wd, yA, yB, yH, mlg);
  final_sum<<<NB * 16, 256, 0, stream>>>(x, yA, yB, yH, mlg, (bf16*)d_out);
}
```

```cpp
#include <hip/hip_runtime.h>
#include <hip/hip_bf16.h>
#include <stdint.h>

typedef unsigned int u32;
typedef __attribute__((ext_vector_type(4))) unsigned int u32x4;
typedef __attribute__((ext_vector_type(4))) float f32x4;
typedef __attribute__((ext_vector_type(2))) __bf16 bf16x2;
typedef __hip_bfloat16 bf16;

constexpr int NB = 8, DM = 512, NH = 32, SK = 4096, DH = 128, FF = 1376;
constexpr int NW = 8;
#define UU 4
constexpr int U = UU;
constexpr int NFW = 4;
constexpr int NT = (NW + NFW) * 64;
constexpr int TILES = SK / (NW * 4 * U);
constexpr int XK = 64;
constexpr int UF = XK / (NW * 4);
constexpr int XT = NB * XK / (NW * 4 * U);
static_assert(TILES >= 4 + 2 * XT && (TILES % 2) == 0 && (XT % 2) == 0 && XT * NW * 4 * U == NB * XK, "tiles");
constexpr int WL = 128 / NW;

#define FAIR 0
#define FAIR_D 2
#define MIMIC_SCORE_ROUND 1

__device__ __forceinline__ float bf_lo(u32 x) { return __uint_as_float(x << 16); }
__device__ __forceinline__ float bf_hi(u32 x) { return __uint_as_float(x & 0xffff0000u); }
__device__ __forceinline__ float rbf(float x) { return __bfloat162float(__float2bfloat16(x)); }
__device__ __forceinline__ u32 pack_bf(float lo, float hi) {
  return (u32)__bfloat16_as_ushort(__float2bfloat16(lo)) | ((u32)__bfloat16_as_ushort(__float2bfloat16(hi)) << 16);
}
__device__ __forceinline__ float dot2(u32 a, u32 b, float acc) {
  return __builtin_amdgcn_fdot2_f32_bf16(__builtin_bit_cast(bf16x2, a), __builtin_bit_cast(bf16x2, b), acc, false);
}
template <int CTRL> __device__ __forceinline__ float dpp(float x) {
  return __builtin_bit_cast(float, __builtin_amdgcn_mov_dpp(__builtin_bit_cast(int, x), CTRL, 0xf, 0xf, true));
}
__device__ __forceinline__ float row16_sum(float x) {
  x += dpp<0xB1>(x);
  x += dpp<0x4E>(x);
  x += dpp<0x141>(x);
  x += dpp<0x140>(x);
  return x;
}
__device__ __forceinline__ float xrow_sum(float x) {
  auto s = __builtin_amdgcn_permlane16_swap(__float_as_uint(x), __float_as_uint(x), false, false);
  x = __uint_as_float(s[0]) + __uint_as_float(s[1]);
  auto t = __builtin_amdgcn_permlane32_swap(__float_as_uint(x), __float_as_uint(x), false, false);
  return __uint_as_float(t[0]) + __uint_as_float(t[1]);
}
__device__ __forceinline__ float xrow_max(float x) {
  auto s = __builtin_amdgcn_permlane16_swap(__float_as_uint(x), __float_as_uint(x), false, false);
  x = fmaxf(__uint_as_float(s[0]), __uint_as_float(s[1]));
  auto t = __builtin_amdgcn_permlane32_swap(__float_as_uint(x), __float_as_uint(x), false, false);
  return fmaxf(__uint_as_float(t[0]), __uint_as_float(t[1]));
}
__device__ __forceinline__ float wave_sum(float x) { return xrow_sum(row16_sum(x)); }
__device__ __forceinline__ u32x4 ld_nt(const bf16* p) { return __builtin_nontemporal_load(reinterpret_cast<const u32x4*>(p)); }
__device__ __forceinline__ u32x4 ld16(const bf16* p) { return *reinterpret_cast<const u32x4*>(p); }
#define WQ_NT 1
#define WO_NT 1
#define FFN_NT 1
__device__ __forceinline__ u32x4 ld_wq(const bf16* p) { return WQ_NT ? ld_nt(p) : ld16(p); }
__device__ __forceinline__ u32x4 ld_wo(const bf16* p) { return WO_NT ? ld_nt(p) : ld16(p); }
__device__ __forceinline__ u32x4 ld_ffn(const bf16* base, u32 boff) {
  return __builtin_nontemporal_load(reinterpret_cast<const u32x4*>(reinterpret_cast<const char*>(base) + boff));
}

#define ST_MODE 0
__device__ __forceinline__ void st_part(float* p, f32x4 v) {
  *reinterpret_cast<f32x4*>(p) = v;
}
constexpr int L_WO = 0;
constexpr int L_XN = L_WO + DH * DM / 2;
constexpr int L_QP = L_XN + DM;
constexpr int L_UP = L_XN;
constexpr int L_AC = L_UP + NFW * NB * 64;
constexpr int L_ML = L_AC + NW * DH;
constexpr int L_O = L_ML + NW * 2;
constexpr int L_HB = L_O + 2 * DH;
constexpr int L_FL = L_HB + 32 * NB;
constexpr int L_PG = L_FL + 8;
constexpr int L_WF = L_PG + 8;
constexpr int L_PR = L_WF + 8;
constexpr int L_XA = L_PR + 8;
constexpr int L_GP = L_XA + 256 * NB;
constexpr int L_END = L_GP + NFW * NB * 64;
static_assert(L_QP + NW * DH <= L_AC, "LDS");
constexpr int L_XN2 = L_XA, L_QP2 = L_XN2 + DM, L_AC2 = L_QP2 + NW * DH, L_ML2 = L_AC2 + NW * DH;
static_assert(L_ML2 + NW * 2 <= L_END && L_END * 4 <= 163840 && (L_XA % 4) == 0 && (L_FL % 4) == 0 && (L_O % 4) == 0, "LDS");
constexpr int NFG = (FF + 63) / 64;
#define LAS __attribute__((address_space(3)))

__device__ __forceinline__ void glds16_nt(const void* gsrc, unsigned lds_dst) {
  unsigned keep;
  asm volatile("s_mov_b32 %0, m0\n\ts_mov_b32 m0, %2\n\ts_nop 0\n\tglobal_load_lds_dwordx4 %1, off nt\n\ts_mov_b32 m0, %0"
               : "=&s"(keep) : "v"(gsrc), "s"(lds_dst) : "memory");
}

#define STAMP(i)
#define PIN16(a, o) asm volatile("" : "+v"(a[o]), "+v"(a[o + 1]), "+v"(a[o + 2]), "+v"(a[o + 3]), "+v"(a[o + 4]), "+v"(a[o + 5]), "+v"(a[o + 6]), \
  "+v"(a[o + 7]), "+v"(a[o + 8]), "+v"(a[o + 9]), "+v"(a[o + 10]), "+v"(a[o + 11]), "+v"(a[o + 12]), "+v"(a[o + 13]), "+v"(a[o + 14]), "+v"(a[o + 15]) :: "memory")
#define PIN64(a) do { PIN16(a, 0); PIN16(a, 16); PIN16(a, 32); PIN16(a, 48); } while (0)

template <int CTRL, int N> __device__ __forceinline__ void tr_dpp(float (&acc)[64], bool up) {
#pragma unroll
  for (int k = 0; k < N; ++k) {
    const float keep = up ? acc[k + N] : acc[k];
    const float send = up ? acc[k] : acc[k + N];
    acc[k] = keep + dpp<CTRL>(send);
  }
}
template <int NMIN> __device__ __forceinline__ void treduce(float (&acc)[64], int lane) {
#pragma unroll
  for (int k = 0; k < 32; ++k) {
    auto s = __builtin_amdgcn_permlane32_swap(__float_as_uint(acc[k]), __float_as_uint(acc[k + 32]), false, false);
    acc[k] = __uint_as_float(s[0]) + __uint_as_float(s[1]);
  }
#pragma unroll
  for (int k = 0; k < 16; ++k) {
    auto s = __builtin_amdgcn_permlane16_swap(__float_as_uint(acc[k]), __float_as_uint(acc[k + 16]), false, false);
    acc[k] = __uint_as_float(s[0]) + __uint_as_float(s[1]);
  }
  if (NMIN <= 8) tr_dpp<0x128, 8>(acc, (lane & 8) != 0);
}

template <int U> struct SetT { u32x4 r[2 * U]; };
typedef SetT<U> Set;

template <int U> __device__ __forceinline__ void load_kv(SetT<U>& s, const bf16* kp, const bf16* vp) {
#pragma unroll
  for (int u = 0; u < U; ++u) { s.r[u] = ld_nt(kp + u * 4 * DH); s.r[U + u] = ld_nt(vp + u * 4 * DH); }
}

template <int U> __device__ __forceinline__ void compute_tile(const SetT<U>& s, const u32x4& q, float& m, float& l, float (&acc)[8]) {
  constexpr float LOG2E = 1.4426950408889634f;
  float sc[U];
#pragma unroll
  for (int u = 0; u < U; ++u) {
    float d = dot2(s.r[u][0], q[0], 0.f);
    d = dot2(s.r[u][1], q[1], d);
    d = dot2(s.r[u][2], q[2], d);
    d = dot2(s.r[u][3], q[3], d);
    d = row16_sum(d);
    d = rbf(rbf(d) * 0.08838834764831845f) * LOG2E;
    sc[u] = d;
  }
  float tm = sc[0];
#pragma unroll
  for (int u = 1; u < U; ++u) tm = fmaxf(tm, sc[u]);
  const float mn = fmaxf(m, tm);
  const float alpha = __builtin_amdgcn_exp2f(m - mn);
  m = mn;
  float ps = 0.f;
#pragma unroll
  for (int u = 0; u < U; ++u) { sc[u] = __builtin_amdgcn_exp2f(sc[u] - mn); ps += sc[u]; }
  l = fmaf(l, alpha, ps);
#pragma unroll
  for (int k = 0; k < 8; ++k) acc[k] *= alpha;
#pragma unroll
  for (int u = 0; u < U; ++u) {
#pragma unroll
    for (int k = 0; k < 4; ++k) {
      acc[2 * k] = fmaf(sc[u], bf_lo(s.r[U + u][k]), acc[2 * k]);
      acc[2 * k + 1] = fmaf(sc[u], bf_hi(s.r[U + u][k]), acc[2 * k + 1]);
    }
  }
}

__device__ __forceinline__ void layernorm8(const u32x4& xv, float (&xn)[8]) {
  float xf[8];
#pragma unroll
  for (int k = 0; k < 4; ++k) { xf[2 * k] = bf_lo(xv[k]); xf[2 * k + 1] = bf_hi(xv[k]); }
  float s = 0.f;
#pragma unroll
  for (int k = 0; k < 8; ++k) s += xf[k];
  const float mean = wave_sum(s) * (1.f / DM);
  float s2 = 0.f;
#pragma unroll
  for (int k = 0; k < 8; ++k) { const float d = xf[k] - mean; s2 = fmaf(d, d, s2); }
  const float var = wave_sum(s2) * (1.f / DM);
  const float mb = rbf(mean);
  const float den = rbf(sqrtf(rbf(rbf(var) + 1e-5f)));
#pragma unroll
  for (int k = 0; k < 8; ++k) xn[k] = rbf(rbf(xf[k] - mb) / den);
}
__device__ __forceinline__ void layernorm_to_lds(const u32x4& xv, float* lds_xn, int lane) {
  float xn[8];
  layernorm8(xv, xn);
  *reinterpret_cast<f32x4*>(lds_xn + 8 * lane) = f32x4{xn[0], xn[1], xn[2], xn[3]};
  *reinterpret_cast<f32x4*>(lds_xn + 8 * lane + 4) = f32x4{xn[4], xn[5], xn[6], xn[7]};
  __builtin_amdgcn_s_waitcnt(0xc07f);
  __builtin_amdgcn_wave_barrier();
}

typedef __attribute__((ext_vector_type(2))) float f32x2;
__device__ __forceinline__ void o_to_lds(const float* ac, const float* ml, float* o, int lane, bool whole, bool writer, float* ml_out) {
  float M = ml[0];
#pragma unroll
  for (int w = 1; w < NW; ++w) M = fmaxf(M, ml[2 * w]);
  float L = 0.f;
  f32x2 o2 = {0.f, 0.f};
#pragma unroll
  for (int w = 0; w < NW; ++w) {
    const float e = __builtin_amdgcn_exp2f(ml[2 * w] - M);
    L = fmaf(e, ml[2 * w + 1], L);
    o2 += e * *reinterpret_cast<const f32x2*>(ac + w * DH + 2 * lane);
  }
  if (!whole && writer) *reinterpret_cast<f32x2*>(ml_out) = f32x2{M, L};
  reinterpret_cast<u32*>(o)[lane] = pack_bf(o2[0] / L, o2[1] / L);
  __builtin_amdgcn_s_waitcnt(0xc07f);
  __builtin_amdgcn_wave_barrier();
}
__device__ __forceinline__ void project_fetch(const float* wo_lds, int sl, int lane, u32 (&pw)[64]) {
  const int rg = lane >> 3;
  const float* wl = wo_lds + (16 * rg) * (DM / 2) + 4 * ((8 * sl + (lane & 7)) ^ (8 * (rg & 1)));
#pragma unroll
  for (int i = 0; i < 8; ++i) {
    const u32x4 w0 = *reinterpret_cast<const u32x4*>(wl + (2 * i) * (DM / 2));
    const u32x4 w1 = *reinterpret_cast<const u32x4*>(wl + (2 * i + 1) * (DM / 2));
#pragma unroll
    for (int k = 0; k < 4; ++k) {
      pw[8 * i + 2 * k] = __builtin_amdgcn_perm(w1[k], w0[k], 0x05040100u);
      pw[8 * i + 2 * k + 1] = __builtin_amdgcn_perm(w1[k], w0[k], 0x07060302u);
    }
  }
}
__device__ __forceinline__ void project_finish(const u32 (&pw)[64], const float* o, int sl, int lane, float* __restrict__ dst) {
  float y[8];
#pragma unroll
  for (int k = 0; k < 8; ++k) y[k] = 0.f;
#pragma unroll
  for (int i4 = 0; i4 < 2; ++i4) {
    const u32x4 ov = *reinterpret_cast<const u32x4*>(o + 8 * (lane >> 3) + 4 * i4);
#pragma unroll
    for (int i = 0; i < 4; ++i)
#pragma unroll
      for (int k = 0; k < 8; ++k) y[k] = dot2(ov[i], pw[8 * (4 * i4 + i) + k], y[k]);
  }
#pragma unroll
  for (int k = 0; k < 4; ++k) {
    auto t = __builtin_amdgcn_permlane32_swap(__float_as_uint(y[k]), __float_as_uint(y[k + 4]), false, false);
    y[k] = __uint_as_float(t[0]) + __uint_as_float(t[1]);
  }
#pragma unroll
  for (int k = 0; k < 2; ++k) {
    auto t = __builtin_amdgcn_permlane16_swap(__float_as_uint(y[k]), __float_as_uint(y[k + 2]), false, false);
    y[k] = __uint_as_float(t[0]) + __uint_as_float(t[1]);
  }
  const bool up = (lane & 8) != 0;
  const float yy = (up ? y[1] : y[0]) + dpp<0x128>(up ? y[0] : y[1]);
  dst[64 * sl + 8 * (lane & 7) + 4 * (lane >> 5) + 2 * ((lane >> 4) & 1) + ((lane >> 3) & 1)] = yy;
}
__device__ __forceinline__ void project(const float* wo_lds, const float* o, int sl, int lane, float* __restrict__ dst) {
  u32 pw[64];
  project_fetch(wo_lds, sl, lane, pw);
  project_finish(pw, o, sl, lane, dst);
}

__global__ void __launch_bounds__(NT) attn_ffn_main(const bf16* __restrict__ x, const bf16* __restrict__ kc,
                                                    const bf16* __restrict__ vc, const bf16* __restrict__ wq,
                                                    const bf16* __restrict__ wo, const bf16* __restrict__ wg,
                                                    const bf16* __restrict__ wu, const bf16* __restrict__ wd,
                                                    float* __restrict__ yA, float* __restrict__ yB,
                                                    float* __restrict__ yH, float* __restrict__ mlg) {
  __shared__ __attribute__((aligned(16))) float lds[L_END];
  const int id = blockIdx.x, b = id >> 5, h = id & 31;
  const int tid = threadIdx.x, lane = tid & 63;
  const int wave = __builtin_amdgcn_readfirstlane(tid >> 6);
  const int r = lane >> 4, c = lane & 15;
  const int he = h >> 1, bh = he & 7;
  const bool hostgrp = (h & 1) == 0, host = hostgrp && b == bh, helper = hostgrp && b != bh, dbl = he < NFG - 16;
  STAMP(0);

  if (wave >= NW) {
    const int fw = wave - NW;
    const bool do_ffn = host;
    if (do_ffn) {
#define FFN_PRIO 3
      __builtin_amdgcn_s_setprio(FFN_PRIO);
      const u32x4 xs0 = ld16(x + (2 * fw) * DM + (u32)(8 * lane));
      const u32x4 xs1 = ld16(x + (2 * fw + 1) * DM + (u32)(8 * lane));
      auto gate_off = [&](int j) {
        const int c0 = 64 * j + 8 * (lane & 7);
        return 2u * (u32)((128 * fw + 2 * (lane >> 3)) * FF + (c0 < FF ? c0 : c0 - 32));
      };
      u32 goff = gate_off(he);
      u32x4 w[16];
      auto load_gate = [&]() {
#pragma unroll
        for (int i = 0; i < 16; ++i) w[i] = ld_ffn(wg + (16 * (i >> 1) + (i & 1)) * FF, goff);
      };
      load_gate();
      __builtin_amdgcn_sched_barrier(0);
      {
        float xn0[8], xn1[8];
        layernorm8(xs0, xn0);
        layernorm8(xs1, xn1);
        typedef __attribute__((ext_vector_type(2))) unsigned int u32x2;
#pragma unroll
        for (int k = 0; k < 4; ++k)
          *reinterpret_cast<u32x2*>(lds + L_XA + (4 * lane + k) * NB + 2 * fw) =
              u32x2{pack_bf(xn0[2 * k], xn0[2 * k + 1]), pack_bf(xn1[2 * k], xn1[2 * k + 1])};
        __builtin_amdgcn_s_waitcnt(0xc07f);
      }
      __builtin_amdgcn_sched_barrier(0);
      __builtin_amdgcn_s_barrier();
      STAMP(1);
      float acc[64];
      auto consume = [&](const float* coef, int cstride, bool down, u32 sel_a, u32 sel_b, auto next) {
#pragma unroll
        for (int k = 0; k < 64; ++k) acc[k] = 0.f;
#pragma unroll
        for (int i2 = 0; i2 < 8; ++i2) {
          const u32x4 xa0 = *reinterpret_cast<const u32x4*>(coef + i2 * cstride);
          const u32x4 xa1 = *reinterpret_cast<const u32x4*>(coef + i2 * cstride + 4);
          const u32 sel = i2 < 4 ? sel_a : sel_b, sel2 = sel + (sel == 0x0c0c0c0cu ? 0u : 0x02020202u);
#pragma unroll
          for (int cc = 0; cc < 8; ++cc) {
            const u32 pw = __builtin_amdgcn_perm(w[2 * i2 + 1][cc >> 1], w[2 * i2][cc >> 1], (cc & 1) ? sel2 : sel);
#pragma unroll
            for (int bb = 0; bb < 8; ++bb) {
              const int idx = down ? ((bb >> 2) * 32 + (cc >> 2) * 16 + (bb & 3) * 4 + (cc & 3)) : (bb * 8 + cc);
              acc[idx] = dot2(bb < 4 ? xa0[bb & 3] : xa1[bb & 3], pw, acc[idx]);
            }
            if (cc & 1) __builtin_amdgcn_sched_barrier(0);
          }
          next(2 * i2);
          next(2 * i2 + 1);
          PIN64(acc);
        }
      };
      constexpr u32 SEL = 0x05040100u, SEL0 = 0x0c0c0c0cu;
      const float* xa = lds + L_XA + (64 * fw + (lane >> 3)) * NB;
      volatile LAS unsigned* fl = (volatile LAS unsigned*)(lds + L_FL);
      volatile LAS unsigned* pg = (volatile LAS unsigned*)(lds + L_PG);
      auto ffn_pass = [&](const unsigned pass) __attribute__((always_inline)) {
        const int jj = he + 16 * (int)pass;
        consume(xa, 8 * NB, false, SEL, SEL, [&](int i) { w[i] = ld_ffn(wu + (16 * (i >> 1) + (i & 1)) * FF, goff); });
        treduce<8>(acc, lane);
        if (pass) while (min(min(fl[4], fl[5]), min(fl[6], fl[7])) < pass) __builtin_amdgcn_s_sleep(2);
        asm volatile("" ::: "memory");
        *reinterpret_cast<f32x4*>(lds + L_GP + fw * 512 + 8 * lane) = f32x4{acc[0], acc[1], acc[2], acc[3]};
        *reinterpret_cast<f32x4*>(lds + L_GP + fw * 512 + 8 * lane + 4) = f32x4{acc[4], acc[5], acc[6], acc[7]};
        STAMP(4);
        const bool full = 64 * jj + 64 <= FF;
        const u32 doff = 2u * (u32)(2 * (lane >> 4) * DM + 128 * fw + 8 * (lane & 15));
        const bf16* wd0 = wd + (size_t)(64 * jj) * DM;
        const bf16* wd1 = wd0 + (full ? 32 * DM : 0);
        consume(xa, 8 * NB, false, SEL, SEL, [&](int i) { w[i] = ld_ffn((i < 8 ? wd0 : wd1) + (8 * ((i >> 1) & 3) + (i & 1)) * DM, doff); });
        treduce<8>(acc, lane);
        if (!pass) while ((pg[0] & pg[1] & pg[2] & pg[3] & pg[4] & pg[5] & pg[6] & pg[7]) == 0u) __builtin_amdgcn_s_sleep(2);
        asm volatile("" ::: "memory");
        *reinterpret_cast<f32x4*>(lds + L_UP + fw * 512 + 8 * lane) = f32x4{acc[0], acc[1], acc[2], acc[3]};
        *reinterpret_cast<f32x4*>(lds + L_UP + fw * 512 + 8 * lane + 4) = f32x4{acc[4], acc[5], acc[6], acc[7]};
        STAMP(5);
        asm volatile("s_waitcnt lgkmcnt(0)" ::: "memory");
        if (lane == 0) fl[fw] = pass + 1;
        while (min(min(fl[0], fl[1]), min(fl[2], fl[3])) <= pass) __builtin_amdgcn_s_sleep(2);
        asm volatile("" ::: "memory");
        __builtin_amdgcn_sched_barrier(0);
        STAMP(2);
        {
          f32x4 g0 = {0.f, 0.f, 0.f, 0.f}, g1 = g0, u0 = g0, u1 = g0;
#pragma unroll
          for (int v = 0; v < NFW; ++v) {
            g0 += *reinterpret_cast<const f32x4*>(lds + L_GP + v * 512 + 8 * lane);
            g1 += *reinterpret_cast<const f32x4*>(lds + L_GP + v * 512 + 8 * lane + 4);
            u0 += *reinterpret_cast<const f32x4*>(lds + L_UP + v * 512 + 8 * lane);
            u1 += *reinterpret_cast<const f32x4*>(lds + L_UP + v * 512 + 8 * lane + 4);
          }
          const bool cvalid = 64 * jj + 8 * (lane & 7) < FF;
          float hv[8];
#pragma unroll
          for (int k = 0; k < 8; ++k) {
            const float gg = rbf(k < 4 ? g0[k & 3] : g1[k & 3]), uu = rbf(k < 4 ? u0[k & 3] : u1[k & 3]);
            hv[k] = cvalid ? (gg * (gg > 0.f ? 1.f : 0.f)) * uu : 0.f;
          }
          u32* hb = reinterpret_cast<u32*>(lds + L_HB) + (4 * (lane & 7)) * NB + (lane >> 3);
#pragma unroll
          for (int k = 0; k < 4; ++k) hb[k * NB] = pack_bf(hv[2 * k], hv[2 * k + 1]);
          __builtin_amdgcn_s_waitcnt(0xc07f);
          __builtin_amdgcn_wave_barrier();
          asm volatile("" ::: "memory");
          if (lane == 0) fl[4 + fw] = pass + 1;
          __builtin_amdgcn_sched_barrier(0);
        }
        consume(lds + L_HB + (lane >> 4) * NB, 4 * NB, true, SEL, full ? SEL : SEL0, [&](int) {});
        treduce<16>(acc, lane);
        STAMP(6);
        float* yp = yB + (size_t)jj * (NB * DM) + (size_t)(lane >> 5) * (4 * DM) + 128 * fw + 8 * (lane & 15) + 4 * ((lane >> 4) & 1);
#pragma unroll
        for (int bb = 0; bb < 4; ++bb)
          st_part(yp + bb * DM, f32x4{acc[bb * 4], acc[bb * 4 + 1], acc[bb * 4 + 2], acc[bb * 4 + 3]});
      };
      ffn_pass(0);
      if (dbl) {
        goff = gate_off(he + 16);
        load_gate();
        ffn_pass(1);
      }
    }
    if (helper) {
      __builtin_amdgcn_s_barrier();
      volatile LAS unsigned* pg = (volatile LAS unsigned*)(lds + L_PG);
      while ((pg[0] & pg[1] & pg[2] & pg[3] & pg[4] & pg[5] & pg[6] & pg[7]) == 0u) __builtin_amdgcn_s_sleep(8);
      asm volatile("" ::: "memory");
      o_to_lds(lds + L_AC2, lds + L_ML2, lds + L_O + DH, lane, false, tid == NW * 64, mlg + (he * NB + b) * 2);
      project(lds + L_WO, lds + L_O + DH, 2 * fw, lane, yH + (size_t)(he * NB + b) * DM);
      project(lds + L_WO, lds + L_O + DH, 2 * fw + 1, lane, yH + (size_t)(he * NB + b) * DM);
    }
    STAMP(3);
    return;
  }

  const u32x4 xv = ld16(x + b * DM + 8 * lane);
  const u32x4 xv2 = ld16(x + bh * DM + 8 * lane);
  Set A, Bs;
  u32x4 W[WL];
  const bf16* kp = kc + (size_t)id * (SK * DH) + (size_t)wave * (4 * U * DH) + r * DH + 8 * c;
  const bf16* vp = vc + (size_t)id * (SK * DH) + (size_t)wave * (4 * U * DH) + r * DH + 8 * c;
  constexpr int TSTRIDE = NW * 4 * U * DH;
  {
    const bf16* wqp = wq + (size_t)(wave * 4 + r) * (NH * DH) + h * DH + 8 * c;
#define ORDER 1
#define LDW(i) W[i] = ld_wq(wqp + (size_t)(i) * (NW * 4) * (NH * DH))
#pragma unroll
    for (int u = 0; u < U; ++u) {
      Bs.r[u] = ld_nt(kp + u * 4 * DH);
      LDW(4 * u); LDW(4 * u + 1);
      Bs.r[U + u] = ld_nt(vp + u * 4 * DH);
      LDW(4 * u + 2); LDW(4 * u + 3);
      __builtin_amdgcn_sched_barrier(0);
    }
    load_kv(A, kp + TSTRIDE, vp + TSTRIDE);
    __builtin_amdgcn_sched_barrier(0);
    if (tid < 2 * NFW + 3 * NW) lds[L_FL + tid] = 0.f;
  }

  layernorm_to_lds(xv, lds + L_XN, lane);
  if (helper) layernorm_to_lds(xv2, lds + L_XN2, lane);
  STAMP(1);
  {
    const float* xn2 = lds + (helper ? L_XN2 : L_XN);
    float qa[8], qb[8];
#pragma unroll
    for (int k = 0; k < 8; ++k) qa[k] = qb[k] = 0.f;
#pragma unroll
    for (int i = 0; i < WL; ++i) {
      const float xr = lds[L_XN + wave * 4 + r + i * (NW * 4)], xr2 = xn2[wave * 4 + r + i * (NW * 4)];
#pragma unroll
      for (int k = 0; k < 4; ++k) {
        const float w0 = bf_lo(W[i][k]), w1 = bf_hi(W[i][k]);
        qa[2 * k] = fmaf(xr, w0, qa[2 * k]);
        qa[2 * k + 1] = fmaf(xr, w1, qa[2 * k + 1]);
        qb[2 * k] = fmaf(xr2, w0, qb[2 * k]);
        qb[2 * k + 1] = fmaf(xr2, w1, qb[2 * k + 1]);
      }
    }
#pragma unroll
    for (int k = 0; k < 8; ++k) {
      auto s16 = __builtin_amdgcn_permlane16_swap(__float_as_uint(qa[k]), __float_as_uint(qb[k]), false, false);
      const float t = __uint_as_float(s16[0]) + __uint_as_float(s16[1]);
      auto s32 = __builtin_amdgcn_permlane32_swap(__float_as_uint(t), __float_as_uint(t), false, false);
      qa[k] = __uint_as_float(s32[0]) + __uint_as_float(s32[1]);
    }
    if (r == 0 || (r == 1 && helper)) {
      float* qp = lds + (r == 0 ? L_QP : L_QP2) + wave * DH + 8 * c;
      *reinterpret_cast<f32x4*>(qp) = f32x4{qa[0], qa[1], qa[2], qa[3]};
      *reinterpret_cast<f32x4*>(qp + 4) = f32x4{qa[4], qa[5], qa[6], qa[7]};
    }
  }
  auto q_final = [&](const float* qp) {
    f32x4 s0 = {0.f, 0.f, 0.f, 0.f}, s1 = {0.f, 0.f, 0.f, 0.f};
#pragma unroll
    for (int w = 0; w < NW; ++w) {
      s0 += *reinterpret_cast<const f32x4*>(qp + w * DH + 8 * c);
      s1 += *reinterpret_cast<const f32x4*>(qp + w * DH + 8 * c + 4);
    }
    return u32x4{pack_bf(s0[0], s0[1]), pack_bf(s0[2], s0[3]), pack_bf(s1[0], s1[1]), pack_bf(s1[2], s1[3])};
  };
  __syncthreads();
  u32x4 q = q_final(lds + L_QP);
  asm volatile("" : "+v"(q));
  if (host) {
    asm volatile("s_waitcnt lgkmcnt(0)" ::: "memory");
    if (lane == 0) ((volatile LAS unsigned*)(lds + L_PG))[wave] = 1u;
  }

  STAMP(2);
  {
  const bf16* wop = wo + (size_t)(h * DH + wave * WL) * DM + (u32)(8 * (lane ^ (8 * (wave & 1))));
  const unsigned wo_lds = (unsigned)(size_t)(LAS float*)(lds + L_WO) + (unsigned)(wave * WL * DM * 2);
#pragma unroll
  for (int i = 0; i < WL; ++i) glds16_nt(wop + (size_t)i * DM, wo_lds + i * (DM * 2));
  }
  SetT<UF> F, F1;
  if (hostgrp) {
    const int xk = dbl ? 2 * XK : XK;
    const size_t fo = (size_t)(bh * NH + h) * (SK * DH) + (size_t)(SK - NB * xk + xk * b + (xk / NW) * wave + r) * DH + 8 * c;
    load_kv(F, kc + fo, vc + fo);
    if (dbl) load_kv(F1, kc + fo + 4 * UF * DH, vc + fo + 4 * UF * DH);
  }
  auto wave_state_to_lds = [&](float m, float l, float (&acc)[8], float* ac, float* ml) {
    const float mw = xrow_max(m);
    const float sc = __builtin_amdgcn_exp2f(m - mw);
    l = xrow_sum(l * sc);
#pragma unroll
    for (int k = 0; k < 8; ++k) acc[k] = xrow_sum(acc[k] * sc);
    if (r == 0) {
      *reinterpret_cast<f32x4*>(ac + wave * DH + 8 * c) = f32x4{acc[0], acc[1], acc[2], acc[3]};
      *reinterpret_cast<f32x4*>(ac + wave * DH + 8 * c + 4) = f32x4{acc[4], acc[5], acc[6], acc[7]};
      if (c == 0) { ml[2 * wave] = mw; ml[2 * wave + 1] = l; }
    }
  };
  float m = -INFINITY, l = 0.f, acc[8];
#pragma unroll
  for (int k = 0; k < 8; ++k) acc[k] = 0.f;
  compute_tile(Bs, q, m, l, acc);
  load_kv(Bs, kp + (size_t)2 * TSTRIDE, vp + (size_t)2 * TSTRIDE);
  compute_tile(A, q, m, l, acc);
  load_kv(A, kp + (size_t)3 * TSTRIDE, vp + (size_t)3 * TSTRIDE);
  if (host) {
    compute_tile(F, q, m, l, acc);
    if (dbl) compute_tile(F1, q, m, l, acc);
  } else if (helper) {
    const u32x4 q2 = q_final(lds + L_QP2);
    float m2 = -INFINITY, l2 = 0.f, acc2[8];
#pragma unroll
    for (int k = 0; k < 8; ++k) acc2[k] = 0.f;
    compute_tile(F, q2, m2, l2, acc2);
    if (dbl) compute_tile(F1, q2, m2, l2, acc2);
    wave_state_to_lds(m2, l2, acc2, lds + L_AC2, lds + L_ML2);
    asm volatile("s_waitcnt lgkmcnt(0)" ::: "memory");
    if (lane == 0) ((volatile LAS unsigned*)(lds + L_PG))[wave] = 1u;
  }
  const int tend = TILES - 2 - (host ? (dbl ? 2 * XT : XT) : 0);
  volatile LAS unsigned* wf = (volatile LAS unsigned*)(lds + L_WF);
  asm volatile("s_waitcnt vmcnt(%0)" :: "n"(4 * U) : "memory");
  if (lane == 0) wf[wave] = 1u;
#define SB __builtin_amdgcn_sched_barrier(0)
#pragma unroll 1
  for (int t = 2; t < tend; t += 2) {
    compute_tile(Bs, q, m, l, acc);
    SB;
    load_kv(Bs, kp + (size_t)(t + 2) * TSTRIDE, vp + (size_t)(t + 2) * TSTRIDE);
    SB;
    compute_tile(A, q, m, l, acc);
    SB;
    load_kv(A, kp + (size_t)(t + 3) * TSTRIDE, vp + (size_t)(t + 3) * TSTRIDE);
    SB;
  }
  u32 pw[64];
  while ((wf[0] & wf[1] & wf[2] & wf[3] & wf[4] & wf[5] & wf[6] & wf[7]) == 0u) __builtin_amdgcn_s_sleep(2);
  asm volatile("" ::: "memory");
  project_fetch(lds + L_WO, wave, lane, pw);
  PIN64(pw);
  compute_tile(Bs, q, m, l, acc);
  compute_tile(A, q, m, l, acc);

  STAMP(3);
  wave_state_to_lds(m, l, acc, lds + L_AC, lds + L_ML);
  __syncthreads();
  o_to_lds(lds + L_AC, lds + L_ML, lds + L_O, lane, !host, tid == 0, mlg + (he * NB + b) * 2);
  project_finish(pw, lds + L_O, wave, lane, yA + (size_t)id * DM);
  STAMP(4);
}

__device__ __forceinline__ float piece_weight(const float* __restrict__ mlg, int hh, int p) {
  f32x4 v[4];
#pragma unroll
  for (int i = 0; i < 4; ++i) v[i] = *reinterpret_cast<const f32x4*>(mlg + hh * (NB * 2) + 4 * i);
  float M = v[0][0];
#pragma unroll
  for (int i = 0; i < 4; ++i) M = fmaxf(M, fmaxf(v[i][0], v[i][2]));
  float L = 0.f, own = 0.f;
#pragma unroll
  for (int i = 0; i < 4; ++i) {
    const float e0 = __builtin_amdgcn_exp2f(v[i][0] - M) * v[i][1], e1 = __builtin_amdgcn_exp2f(v[i][2] - M) * v[i][3];
    L += e0; L += e1;
    own = p == 2 * i ? e0 : (p == 2 * i + 1 ? e1 : own);
  }
  return own / L;
}

__global__ void __launch_bounds__(256) final_sum(const bf16* __restrict__ x, const float* __restrict__ yA,
                                                 const float* __restrict__ yB, const float* __restrict__ yH,
                                                 const float* __restrict__ mlg, bf16* __restrict__ out) {
  __shared__ __attribute__((aligned(16))) float red[2][4][32];
  const int b = blockIdx.x >> 4, cb = blockIdx.x & 15;
  const int tid = threadIdx.x, c4 = tid & 7, g = tid >> 3;
  const int col = cb * 32 + 4 * c4;
  const f32x4 zero = {0.f, 0.f, 0.f, 0.f};
  const int hh = b + 8 * (g >> 3), p = g & 7;
  const bool split = (g & 1) == 0 && ((g >> 1) & 7) == b, piece = g < 16 && p != b;
  const int n = b * DM + cb * 32 + (tid & 31);
  const float xv = __bfloat162float(x[n]);
  f32x4 va = *reinterpret_cast<const f32x4*>(yA + (size_t)(b * NH + g) * DM + col);
  f32x4 sb = g < NFG ? *reinterpret_cast<const f32x4*>(yB + (size_t)(g * NB + b) * DM + col) : zero;
  const f32x4 vh = piece ? *reinterpret_cast<const f32x4*>(yH + (size_t)(hh * NB + p) * DM + col) : zero;
  if (split) va *= piece_weight(mlg, g >> 1, b);
  if (piece) va += vh * piece_weight(mlg, hh, p);
#pragma unroll
  for (int k = 0; k < 4; ++k) {
    va[k] = xrow_sum(va[k] + dpp<0x128>(va[k]));
    sb[k] = xrow_sum(sb[k] + dpp<0x128>(sb[k]));
  }
  if ((tid & 63) < 8) {
    *reinterpret_cast<f32x4*>(&red[0][tid >> 6][4 * c4]) = va;
    *reinterpret_cast<f32x4*>(&red[1][tid >> 6][4 * c4]) = sb;
  }
  __syncthreads();
  if (tid < 32) {
    const float a = (red[0][0][tid] + red[0][1][tid]) + (red[0][2][tid] + red[0][3][tid]);
    const float f = (red[1][0][tid] + red[1][1][tid]) + (red[1][2][tid] + red[1][3][tid]);
    out[n] = __float2bfloat16(rbf(xv + rbf(a)) + rbf(f));
  }
}

extern "C" void kernel_launch(void* const* d_in, const int* in_sizes, int n_in,
                              void* d_out, int out_size, void* d_ws, size_t ws_size,
                              hipStream_t stream) {
  const bf16* x = (const bf16*)d_in[0];
  const bf16* kc = (const bf16*)d_in[1];
  const bf16* vc = (const bf16*)d_in[2];
  const bf16* wq = (const bf16*)d_in[3];
  const bf16* wo = (const bf16*)d_in[4];
  const bf16* wg = (const bf16*)d_in[6];
  const bf16* wu = (const bf16*)d_in[7];
  const bf16* wd = (const bf16*)d_in[8];
  float* yA = (float*)d_ws;
  float* yB = yA + NB * NH * DM;
  float* yH = yB + NFG * NB * DM;
  float* mlg = yH + 16 * NB * DM;
  attn_ffn_main<<<NB * NH, NT, 0, stream>>>(x, kc, vc, wq, wo, wg, wu, wd, yA, yB, yH, mlg);
  final_sum<<<NB * 16, 256, 0, stream>>>(x, yA, yB, yH, mlg, (bf16*)d_out);
}
```

```cpp
#include <hip/hip_runtime.h>
#include <hip/hip_bf16.h>
#include <stdint.h>

typedef unsigned int u32;
typedef __attribute__((ext_vector_type(4))) unsigned int u32x4;
typedef __attribute__((ext_vector_type(2))) unsigned int u32x2;
typedef __attribute__((ext_vector_type(4))) float f32x4;
typedef __attribute__((ext_vector_type(2))) __bf16 bf16x2;
typedef __hip_bfloat16 bf16;

constexpr int NB = 8, DM = 512, NH = 32, SK = 4096, DH = 128, FF = 1376;
constexpr int NW = 8;
constexpr int U = 4;
constexpr int NFW = 4;
constexpr int NT = (NW + NFW) * 64;
constexpr int TILES = SK / (NW * 4 * U);
constexpr int XK = 64;
constexpr int UF = XK / (NW * 4);
constexpr int XT = NB * XK / (NW * 4 * U);
static_assert(NW == 8 && U == 4 && TILES >= 4 + 2 * XT && (TILES % 2) == 0 && (XT % 2) == 0 && XT * NW * 4 * U == NB * XK, "tiles");
constexpr int WL = 128 / NW;

__device__ __forceinline__ float bf_lo(u32 x) { return __uint_as_float(x << 16); }
__device__ __forceinline__ float bf_hi(u32 x) { return __uint_as_float(x & 0xffff0000u); }
__device__ __forceinline__ float rbf(float x) { return __bfloat162float(__float2bfloat16(x)); }
__device__ __forceinline__ u32 pack_bf(float lo, float hi) {
  return (u32)__bfloat16_as_ushort(__float2bfloat16(lo)) | ((u32)__bfloat16_as_ushort(__float2bfloat16(hi)) << 16);
}
__device__ __forceinline__ float dot2(u32 a, u32 b, float acc) {
  return __builtin_amdgcn_fdot2_f32_bf16(__builtin_bit_cast(bf16x2, a), __builtin_bit_cast(bf16x2, b), acc, false);
}
template <int CTRL> __device__ __forceinline__ float dpp(float x) {
  return __builtin_bit_cast(float, __builtin_amdgcn_mov_dpp(__builtin_bit_cast(int, x), CTRL, 0xf, 0xf, true));
}
__device__ __forceinline__ float row16_sum(float x) {
  x += dpp<0xB1>(x);
  x += dpp<0x4E>(x);
  x += dpp<0x141>(x);
  x += dpp<0x140>(x);
  return x;
}
__device__ __forceinline__ float xrow_sum(float x) {
  auto s = __builtin_amdgcn_permlane16_swap(__float_as_uint(x), __float_as_uint(x), false, false);
  x = __uint_as_float(s[0]) + __uint_as_float(s[1]);
  auto t = __builtin_amdgcn_permlane32_swap(__float_as_uint(x), __float_as_uint(x), false, false);
  return __uint_as_float(t[0]) + __uint_as_float(t[1]);
}
__device__ __forceinline__ float xrow_max(float x) {
  auto s = __builtin_amdgcn_permlane16_swap(__float_as_uint(x), __float_as_uint(x), false, false);
  x = fmaxf(__uint_as_float(s[0]), __uint_as_float(s[1]));
  auto t = __builtin_amdgcn_permlane32_swap(__float_as_uint(x), __float_as_uint(x), false, false);
  return fmaxf(__uint_as_float(t[0]), __uint_as_float(t[1]));
}
__device__ __forceinline__ float wave_sum(float x) { return xrow_sum(row16_sum(x)); }
__device__ __forceinline__ u32x4 ld_nt(const bf16* p) { return __builtin_nontemporal_load(reinterpret_cast<const u32x4*>(p)); }
__device__ __forceinline__ u32x4 ld16(const bf16* p) { return *reinterpret_cast<const u32x4*>(p); }
__device__ __forceinline__ u32x4 ld_ffn(const bf16* base, u32 boff) {
  return __builtin_nontemporal_load(reinterpret_cast<const u32x4*>(reinterpret_cast<const char*>(base) + boff));
}

constexpr int L_WO = 0;
constexpr int L_XN = L_WO + DH * DM / 2;
constexpr int L_QP = L_XN + DM;
constexpr int L_UP = L_XN;
constexpr int L_AC = L_UP + NFW * NB * 64;
constexpr int L_ML = L_AC + NW * DH;
constexpr int L_O = L_ML + NW * 2;
constexpr int L_HB = L_O + 2 * DH;
constexpr int L_FL = L_HB + 32 * NB;
constexpr int L_PG = L_FL + 8;
constexpr int L_WF = L_PG + 8;
constexpr int L_TK = L_WF + 4;
constexpr int L_XA = L_TK + 4;
constexpr int L_GP = L_XA + 256 * NB;
constexpr int L_END = L_GP + NFW * NB * 64;
static_assert(L_QP + NW * DH <= L_AC, "LDS");
constexpr int L_XN2 = L_XA, L_QP2 = L_XN2 + DM, L_AC2 = L_QP2 + NW * DH, L_ML2 = L_AC2 + NW * DH;
static_assert(L_ML2 + NW * 2 <= L_END && L_END * 4 <= 163840 && (L_XA % 4) == 0 && (L_FL % 4) == 0 && (L_O % 4) == 0, "LDS");
constexpr int NFG = (FF + 63) / 64;
#define LAS __attribute__((address_space(3)))

__device__ __forceinline__ void glds16_nt(const void* gsrc, unsigned lds_dst) {
  unsigned keep;
  asm volatile("s_mov_b32 %0, m0\n\ts_mov_b32 m0, %2\n\ts_nop 0\n\tglobal_load_lds_dwordx4 %1, off nt\n\ts_mov_b32 m0, %0"
               : "=&s"(keep) : "v"(gsrc), "s"(lds_dst) : "memory");
}

#define PIN16(a, o) asm volatile("" : "+v"(a[o]), "+v"(a[o + 1]), "+v"(a[o + 2]), "+v"(a[o + 3]), "+v"(a[o + 4]), "+v"(a[o + 5]), "+v"(a[o + 6]), \
  "+v"(a[o + 7]), "+v"(a[o + 8]), "+v"(a[o + 9]), "+v"(a[o + 10]), "+v"(a[o + 11]), "+v"(a[o + 12]), "+v"(a[o + 13]), "+v"(a[o + 14]), "+v"(a[o + 15]) :: "memory")
#define PIN64(a) do { PIN16(a, 0); PIN16(a, 16); PIN16(a, 32); PIN16(a, 48); } while (0)

template <int CTRL, int N> __device__ __forceinline__ void tr_dpp(float (&acc)[64], bool up) {
#pragma unroll
  for (int k = 0; k < N; ++k) {
    const float keep = up ? acc[k + N] : acc[k];
    const float send = up ? acc[k] : acc[k + N];
    acc[k] = keep + dpp<CTRL>(send);
  }
}
template <int NMIN> __device__ __forceinline__ void treduce(float (&acc)[64], int lane) {
#pragma unroll
  for (int k = 0; k < 32; ++k) {
    auto s = __builtin_amdgcn_permlane32_swap(__float_as_uint(acc[k]), __float_as_uint(acc[k + 32]), false, false);
    acc[k] = __uint_as_float(s[0]) + __uint_as_float(s[1]);
  }
#pragma unroll
  for (int k = 0; k < 16; ++k) {
    auto s = __builtin_amdgcn_permlane16_swap(__float_as_uint(acc[k]), __float_as_uint(acc[k + 16]), false, false);
    acc[k] = __uint_as_float(s[0]) + __uint_as_float(s[1]);
  }
  if (NMIN <= 8) tr_dpp<0x128, 8>(acc, (lane & 8) != 0);
}

template <int U> struct SetT { u32x4 r[2 * U]; };
typedef SetT<U> Set;

template <int U> __device__ __forceinline__ void load_kv(SetT<U>& s, const bf16* kp, const bf16* vp) {
#pragma unroll
  for (int u = 0; u < U; ++u) { s.r[u] = ld_nt(kp + u * 4 * DH); s.r[U + u] = ld_nt(vp + u * 4 * DH); }
}

__device__ __forceinline__ void load_tile(Set& s, const bf16* kb, const bf16* vb, int t, u32 lo) {
#pragma unroll
  for (int u = 0; u < U; ++u) {
    const size_t o = (size_t)((t >> 1) * 256 + (t & 1) * 16 + u * 64) * DH;
    s.r[u] = ld_ffn(kb + o, lo); s.r[U + u] = ld_ffn(vb + o, lo);
  }
}

template <int U> __device__ __forceinline__ void compute_tile(const SetT<U>& s, const u32x4& q, float& m, float& l, float (&acc)[8]) {
  constexpr float LOG2E = 1.4426950408889634f;
  float sc[U];
#pragma unroll
  for (int u = 0; u < U; ++u) {
    float d = dot2(s.r[u][0], q[0], 0.f);
    d = dot2(s.r[u][1], q[1], d);
    d = dot2(s.r[u][2], q[2], d);
    d = dot2(s.r[u][3], q[3], d);
    d = row16_sum(d);
    d = rbf(rbf(d) * 0.08838834764831845f) * LOG2E;
    sc[u] = d;
  }
  float tm = sc[0];
#pragma unroll
  for (int u = 1; u < U; ++u) tm = fmaxf(tm, sc[u]);
  const float mn = fmaxf(m, tm);
  const float alpha = __builtin_amdgcn_exp2f(m - mn);
  m = mn;
  float ps = 0.f;
#pragma unroll
  for (int u = 0; u < U; ++u) { sc[u] = __builtin_amdgcn_exp2f(sc[u] - mn); ps += sc[u]; }
  l = fmaf(l, alpha, ps);
#pragma unroll
  for (int k = 0; k < 8; ++k) acc[k] *= alpha;
#pragma unroll
  for (int u = 0; u < U; ++u) {
#pragma unroll
    for (int k = 0; k < 4; ++k) {
      acc[2 * k] = fmaf(sc[u], bf_lo(s.r[U + u][k]), acc[2 * k]);
      acc[2 * k + 1] = fmaf(sc[u], bf_hi(s.r[U + u][k]), acc[2 * k + 1]);
    }
  }
}

__device__ __forceinline__ void layernorm8(const u32x4& xv, float (&xn)[8]) {
  float xf[8];
#pragma unroll
  for (int k = 0; k < 4; ++k) { xf[2 * k] = bf_lo(xv[k]); xf[2 * k + 1] = bf_hi(xv[k]); }
  float s = 0.f;
#pragma unroll
  for (int k = 0; k < 8; ++k) s += xf[k];
  const float mean = wave_sum(s) * (1.f / DM);
  float s2 = 0.f;
#pragma unroll
  for (int k = 0; k < 8; ++k) { const float d = xf[k] - mean; s2 = fmaf(d, d, s2); }
  const float var = wave_sum(s2) * (1.f / DM);
  const float mb = rbf(mean);
  const float den = rbf(sqrtf(rbf(rbf(var) + 1e-5f)));
#pragma unroll
  for (int k = 0; k < 8; ++k) xn[k] = rbf(rbf(xf[k] - mb) / den);
}
__device__ __forceinline__ void layernorm_to_lds(const u32x4& xv, float* lds_xn, int lane) {
  float xn[8];
  layernorm8(xv, xn);
  *reinterpret_cast<f32x4*>(lds_xn + 8 * lane) = f32x4{xn[0], xn[1], xn[2], xn[3]};
  *reinterpret_cast<f32x4*>(lds_xn + 8 * lane + 4) = f32x4{xn[4], xn[5], xn[6], xn[7]};
  __builtin_amdgcn_s_waitcnt(0xc07f);
  __builtin_amdgcn_wave_barrier();
}

typedef __attribute__((ext_vector_type(2))) float f32x2;
__device__ __forceinline__ void o_to_lds(const float* ac, const float* ml, float* o, int lane, bool whole, bool writer, float* ml_out) {
  float M = ml[0];
#pragma unroll
  for (int w = 1; w < NW; ++w) M = fmaxf(M, ml[2 * w]);
  float L = 0.f;
  f32x2 o2 = {0.f, 0.f};
#pragma unroll
  for (int w = 0; w < NW; ++w) {
    const float e = __builtin_amdgcn_exp2f(ml[2 * w] - M);
    L = fmaf(e, ml[2 * w + 1], L);
    o2 += e * *reinterpret_cast<const f32x2*>(ac + w * DH + 2 * lane);
  }
  if (!whole && writer) *reinterpret_cast<f32x2*>(ml_out) = f32x2{M, L};
  reinterpret_cast<u32*>(o)[lane] = pack_bf(o2[0] / L, o2[1] / L);
  __builtin_amdgcn_s_waitcnt(0xc07f);
  __builtin_amdgcn_wave_barrier();
}
__device__ __forceinline__ void project_fetch(const float* wo_lds, int sl, int lane, u32 (&pw)[64]) {
  const int rg = lane >> 3;
  const float* wl = wo_lds + (16 * rg) * (DM / 2) + 4 * ((8 * sl + (lane & 7)) ^ (8 * (rg & 1)));
#pragma unroll
  for (int i = 0; i < 8; ++i) {
    const u32x4 w0 = *reinterpret_cast<const u32x4*>(wl + (2 * i) * (DM / 2));
    const u32x4 w1 = *reinterpret_cast<const u32x4*>(wl + (2 * i + 1) * (DM / 2));
#pragma unroll
    for (int k = 0; k < 4; ++k) {
      pw[8 * i + 2 * k] = __builtin_amdgcn_perm(w1[k], w0[k], 0x05040100u);
      pw[8 * i + 2 * k + 1] = __builtin_amdgcn_perm(w1[k], w0[k], 0x07060302u);
    }
  }
}
__device__ __forceinline__ void project_finish(const u32 (&pw)[64], const float* o, int sl, int lane, float* __restrict__ dst) {
  float y[8];
#pragma unroll
  for (int k = 0; k < 8; ++k) y[k] = 0.f;
#pragma unroll
  for (int i4 = 0; i4 < 2; ++i4) {
    const u32x4 ov = *reinterpret_cast<const u32x4*>(o + 8 * (lane >> 3) + 4 * i4);
#pragma unroll
    for (int i = 0; i < 4; ++i)
#pragma unroll
      for (int k = 0; k < 8; ++k) y[k] = dot2(ov[i], pw[8 * (4 * i4 + i) + k], y[k]);
  }
#pragma unroll
  for (int k = 0; k < 4; ++k) {
    auto t = __builtin_amdgcn_permlane32_swap(__float_as_uint(y[k]), __float_as_uint(y[k + 4]), false, false);
    y[k] = __uint_as_float(t[0]) + __uint_as_float(t[1]);
  }
#pragma unroll
  for (int k = 0; k < 2; ++k) {
    auto t = __builtin_amdgcn_permlane16_swap(__float_as_uint(y[k]), __float_as_uint(y[k + 2]), false, false);
    y[k] = __uint_as_float(t[0]) + __uint_as_float(t[1]);
  }
  const bool up = (lane & 8) != 0;
  const float yy = (up ? y[1] : y[0]) + dpp<0x128>(up ? y[0] : y[1]);
  dst[64 * sl + 8 * (lane & 7) + 4 * (lane >> 5) + 2 * ((lane >> 4) & 1) + ((lane >> 3) & 1)] = yy;
}
__device__ __forceinline__ void project(const float* wo_lds, const float* o, int sl, int lane, float* __restrict__ dst) {
  u32 pw[64];
  project_fetch(wo_lds, sl, lane, pw);
  project_finish(pw, o, sl, lane, dst);
}

__global__ void __launch_bounds__(NT) attn_ffn_main(const bf16* __restrict__ x, const bf16* __restrict__ kc,
                                                    const bf16* __restrict__ vc, const bf16* __restrict__ wq,
                                                    const bf16* __restrict__ wo, const bf16* __restrict__ wg,
                                                    const bf16* __restrict__ wu, const bf16* __restrict__ wd,
                                                    float* __restrict__ yA, float* __restrict__ yB,
                                                    float* __restrict__ yH, float* __restrict__ mlg) {
  __shared__ __attribute__((aligned(16))) float lds[L_END];
  const int id = blockIdx.x, b = id >> 5, h = id & 31;
  const int tid = threadIdx.x, lane = tid & 63;
  const int wave = __builtin_amdgcn_readfirstlane(tid >> 6);
  const int r = lane >> 4, c = lane & 15;
  const int he = h >> 1, bh = he & 7;
  const bool hostgrp = (h & 1) == 0, host = hostgrp && b == bh, helper = hostgrp && b != bh, dbl = he < NFG - 16;

  if (wave >= NW) {
    const int fw = wave - NW;
    const u32 t_aux = (u32)__builtin_amdgcn_s_memrealtime();
    if (fw == 3) { const float tv = yA[(size_t)id * DM + lane]; asm volatile("" :: "v"(tv)); }
    if (host) {
      __builtin_amdgcn_s_setprio(3);
      const u32x4 xs0 = ld16(x + (2 * fw) * DM + (u32)(8 * lane));
      const u32x4 xs1 = ld16(x + (2 * fw + 1) * DM + (u32)(8 * lane));
      auto gate_off = [&](int j) {
        const int c0 = 64 * j + 8 * (lane & 7);
        return 2u * (u32)((128 * fw + 2 * (lane >> 3)) * FF + (c0 < FF ? c0 : c0 - 32));
      };
      u32 goff = gate_off(he);
      u32x4 w[16];
      auto load_gate = [&]() {
#pragma unroll
        for (int i = 0; i < 16; ++i) w[i] = ld_ffn(wg + (16 * (i >> 1) + (i & 1)) * FF, goff);
      };
      load_gate();
      __builtin_amdgcn_sched_barrier(0);
      {
        float xn0[8], xn1[8];
        layernorm8(xs0, xn0);
        layernorm8(xs1, xn1);
#pragma unroll
        for (int k = 0; k < 4; ++k)
          *reinterpret_cast<u32x2*>(lds + L_XA + (4 * lane + k) * NB + 2 * fw) =
              u32x2{pack_bf(xn0[2 * k], xn0[2 * k + 1]), pack_bf(xn1[2 * k], xn1[2 * k + 1])};
        __builtin_amdgcn_s_waitcnt(0xc07f);
      }
      __builtin_amdgcn_sched_barrier(0);
      __builtin_amdgcn_s_barrier();
      float acc[64];
      auto consume = [&](const float* coef, int cstride, bool down, u32 sel_a, u32 sel_b, auto next) {
#pragma unroll
        for (int k = 0; k < 64; ++k) acc[k] = 0.f;
#pragma unroll
        for (int i2 = 0; i2 < 8; ++i2) {
          const u32x4 xa0 = *reinterpret_cast<const u32x4*>(coef + i2 * cstride);
          const u32x4 xa1 = *reinterpret_cast<const u32x4*>(coef + i2 * cstride + 4);
          const u32 sel = i2 < 4 ? sel_a : sel_b, sel2 = sel + (sel == 0x0c0c0c0cu ? 0u : 0x02020202u);
#pragma unroll
          for (int cc = 0; cc < 8; ++cc) {
            const u32 pw = __builtin_amdgcn_perm(w[2 * i2 + 1][cc >> 1], w[2 * i2][cc >> 1], (cc & 1) ? sel2 : sel);
#pragma unroll
            for (int bb = 0; bb < 8; ++bb) {
              const int idx = down ? ((bb >> 2) * 32 + (cc >> 2) * 16 + (bb & 3) * 4 + (cc & 3)) : (bb * 8 + cc);
              acc[idx] = dot2(bb < 4 ? xa0[bb & 3] : xa1[bb & 3], pw, acc[idx]);
            }
            if (cc & 1) __builtin_amdgcn_sched_barrier(0);
          }
          next(2 * i2);
          next(2 * i2 + 1);
          PIN64(acc);
        }
      };
      constexpr u32 SEL = 0x05040100u, SEL0 = 0x0c0c0c0cu;
      const float* xa = lds + L_XA + (64 * fw + (lane >> 3)) * NB;
      volatile LAS unsigned* fl = (volatile LAS unsigned*)(lds + L_FL);
      volatile LAS unsigned* pg = (volatile LAS unsigned*)(lds + L_PG);
      auto ffn_pass = [&](const unsigned pass) __attribute__((always_inline)) {
        const int jj = he + 16 * (int)pass;
        consume(xa, 8 * NB, false, SEL, SEL, [&](int i) { w[i] = ld_ffn(wu + (16 * (i >> 1) + (i & 1)) * FF, goff); });
        treduce<8>(acc, lane);
        if (pass) while (min(min(fl[4], fl[5]), min(fl[6], fl[7])) < pass) __builtin_amdgcn_s_sleep(2);
        asm volatile("" ::: "memory");
        *reinterpret_cast<f32x4*>(lds + L_GP + fw * 512 + 8 * lane) = f32x4{acc[0], acc[1], acc[2], acc[3]};
        *reinterpret_cast<f32x4*>(lds + L_GP + fw * 512 + 8 * lane + 4) = f32x4{acc[4], acc[5], acc[6], acc[7]};
        const bool full = 64 * jj + 64 <= FF;
        const u32 doff = 2u * (u32)(2 * (lane >> 4) * DM + 128 * fw + 8 * (lane & 15));
        const bf16* wd0 = wd + (size_t)(64 * jj) * DM;
        const bf16* wd1 = wd0 + (full ? 32 * DM : 0);
        consume(xa, 8 * NB, false, SEL, SEL, [&](int i) { w[i] = ld_ffn((i < 8 ? wd0 : wd1) + (8 * ((i >> 1) & 3) + (i & 1)) * DM, doff); });
        treduce<8>(acc, lane);
        if (!pass) while ((pg[0] & pg[1] & pg[2] & pg[3] & pg[4] & pg[5] & pg[6] & pg[7]) == 0u) __builtin_amdgcn_s_sleep(2);
        asm volatile("" ::: "memory");
        *reinterpret_cast<f32x4*>(lds + L_UP + fw * 512 + 8 * lane) = f32x4{acc[0], acc[1], acc[2], acc[3]};
        *reinterpret_cast<f32x4*>(lds + L_UP + fw * 512 + 8 * lane + 4) = f32x4{acc[4], acc[5], acc[6], acc[7]};
        asm volatile("s_waitcnt lgkmcnt(0)" ::: "memory");
        if (lane == 0) fl[fw] = pass + 1;
        while (min(min(fl[0], fl[1]), min(fl[2], fl[3])) <= pass) __builtin_amdgcn_s_sleep(2);
        asm volatile("" ::: "memory");
        __builtin_amdgcn_sched_barrier(0);
        {
          f32x4 g0 = {0.f, 0.f, 0.f, 0.f}, g1 = g0, u0 = g0, u1 = g0;
#pragma unroll
          for (int v = 0; v < NFW; ++v) {
            g0 += *reinterpret_cast<const f32x4*>(lds + L_GP + v * 512 + 8 * lane);
            g1 += *reinterpret_cast<const f32x4*>(lds + L_GP + v * 512 + 8 * lane + 4);
            u0 += *reinterpret_cast<const f32x4*>(lds + L_UP + v * 512 + 8 * lane);
            u1 += *reinterpret_cast<const f32x4*>(lds + L_UP + v * 512 + 8 * lane + 4);
          }
          const bool cvalid = 64 * jj + 8 * (lane & 7) < FF;
          float hv[8];
#pragma unroll
          for (int k = 0; k < 8; ++k) {
            const float gg = rbf(k < 4 ? g0[k & 3] : g1[k & 3]), uu = rbf(k < 4 ? u0[k & 3] : u1[k & 3]);
            hv[k] = cvalid ? (gg * (gg > 0.f ? 1.f : 0.f)) * uu : 0.f;
          }
          u32* hb = reinterpret_cast<u32*>(lds + L_HB) + (4 * (lane & 7)) * NB + (lane >> 3);
#pragma unroll
          for (int k = 0; k < 4; ++k) hb[k * NB] = pack_bf(hv[2 * k], hv[2 * k + 1]);
          __builtin_amdgcn_s_waitcnt(0xc07f);
          __builtin_amdgcn_wave_barrier();
          asm volatile("" ::: "memory");
          if (lane == 0) fl[4 + fw] = pass + 1;
          __builtin_amdgcn_sched_barrier(0);
        }
        consume(lds + L_HB + (lane >> 4) * NB, 4 * NB, true, SEL, full ? SEL : SEL0, [&](int) {});
        treduce<16>(acc, lane);
        float* yp = yB + (size_t)jj * (NB * DM) + (size_t)(lane >> 5) * (4 * DM) + 128 * fw + 8 * (lane & 15) + 4 * ((lane >> 4) & 1);
#pragma unroll
        for (int bb = 0; bb < 4; ++bb)
          *reinterpret_cast<f32x4*>(yp + bb * DM) = f32x4{acc[bb * 4], acc[bb * 4 + 1], acc[bb * 4 + 2], acc[bb * 4 + 3]};
      };
      ffn_pass(0);
      if (dbl) {
        goff = gate_off(he + 16);
        load_gate();
        ffn_pass(1);
      }
    }
    {
      if (!host) __builtin_amdgcn_s_barrier();
      __builtin_amdgcn_s_setprio(3);
      const bf16* wop = wo + (size_t)(h * DH + fw * 32) * DM;
      const unsigned wo_lds = (unsigned)(size_t)(LAS float*)(lds + L_WO) + (unsigned)(fw * 32 * DM * 2);
#pragma unroll
      for (int i = 0; i < 32; ++i) {
        glds16_nt(wop + (size_t)i * DM + (u32)(8 * (lane ^ (8 * (i >> 4)))), wo_lds + i * (DM * 2));
        if (!host) __builtin_amdgcn_s_sleep(32);
      }
      volatile LAS unsigned* wf = (volatile LAS unsigned*)(lds + L_WF);
      asm volatile("s_waitcnt vmcnt(0)" ::: "memory");
      if (lane == 0) wf[fw] = 1u;
      if (helper) while ((wf[0] & wf[1] & wf[2] & wf[3]) == 0u) __builtin_amdgcn_s_sleep(8);
    }
    if (helper) {
      volatile LAS unsigned* pg = (volatile LAS unsigned*)(lds + L_PG);
      while ((pg[0] & pg[1] & pg[2] & pg[3] & pg[4] & pg[5] & pg[6] & pg[7]) == 0u) __builtin_amdgcn_s_sleep(8);
      asm volatile("" ::: "memory");
      o_to_lds(lds + L_AC2, lds + L_ML2, lds + L_O + DH, lane, false, tid == NW * 64, mlg + (he * NB + b) * 2);
      project(lds + L_WO, lds + L_O + DH, 2 * fw, lane, yH + (size_t)(he * NB + b) * DM);
      project(lds + L_WO, lds + L_O + DH, 2 * fw + 1, lane, yH + (size_t)(he * NB + b) * DM);
    }
    if (fw == 0 && !host) {
      for (int i = 0; i < 4096 && (u32)__builtin_amdgcn_s_memrealtime() - t_aux < 4000u; ++i) __builtin_amdgcn_s_sleep(16);
      asm volatile("" ::: "memory");
      const u32 sv = __hip_atomic_load(reinterpret_cast<const u32*>(mlg) + 256 + (lane & 31), __ATOMIC_RELAXED, __HIP_MEMORY_SCOPE_AGENT);
      asm volatile("s_waitcnt vmcnt(0)" ::: "memory");
      const u32 now = (u32)__builtin_amdgcn_s_memrealtime(), el = now - t_aux;
      const u32 age = (now - (sv >> 14)) & 0x3ffffu, d = sv & 16383u;
      const bool ok = lane < 32 && age < el && d > 1500u && age + d < el + 300u && age + d + 300u > el;
      const float n = wave_sum(ok ? 1.f : 0.f), sa = wave_sum(ok ? (float)age : 0.f) * (1.f / 30.f);
      const float sd = (wave_sum(ok ? (float)d : 0.f) + (30.f - n) * (float)el) * (1.f / 30.f);
      const float tl = fminf(fmaxf(7580.f + 1.25f * (sd - 3245.f) + 2.6f * fmaxf(sd - 3350.f, 0.f), 7500.f), 8400.f);
      const float rem = n >= 15.f ? tl - sa - sd : 7630.f - (float)el;
      const u32 kn = *(volatile LAS u32*)(lds + L_TK);
      if (kn < 110u && rem > 0.f) {
        const u32 slope = min((u32)(rem * 256.f / (float)(111u - kn)), 100u * 256u);
        if (lane == 0) *(volatile LAS u32x2*)(lds + L_TK + 2) = u32x2{now + (u32)rem - (111u * slope >> 8), slope};
      }
    }
    return;
  }

  const unsigned t_start = (unsigned)__builtin_amdgcn_s_memrealtime();
  const u32x4 xv = ld16(x + b * DM + 8 * lane);
  const u32x4 xv2 = ld16(x + bh * DM + 8 * lane);
  Set A, Bs;
  u32x4 W[WL];
  const bf16* kp = kc + (size_t)id * (SK * DH) + (size_t)wave * DH;
  const bf16* vp = vc + (size_t)id * (SK * DH) + (size_t)wave * DH;
  const u32 lo = 2u * (u32)(((r & 1) * 8 + (r >> 1) * 32) * DH + 8 * c);
  {
    const bf16* wqp = wq + (size_t)(wave * 4 + r) * (NH * DH) + h * DH + 8 * c;
    if (tid < L_XA - L_FL)
      reinterpret_cast<u32*>(lds)[L_FL + tid] = tid == L_TK + 2 - L_FL ? t_start + 100u : tid == L_TK + 3 - L_FL ? 66u * 256u : 0u;
    __builtin_amdgcn_sched_barrier(0);
#pragma unroll
    for (int i = 0; i < WL; ++i) W[i] = ld_nt(wqp + (size_t)i * (NW * 4) * (NH * DH));
    __builtin_amdgcn_sched_barrier(0);
#pragma unroll
    for (int u = 0; u < 2 * U; ++u) Bs.r[u] = ld_ffn((u < U ? kp : vp) + (size_t)((u % U) * 64 * DH), lo);
    __builtin_amdgcn_sched_barrier(0);
  }

  layernorm_to_lds(xv, lds + L_XN, lane);
  if (helper) layernorm_to_lds(xv2, lds + L_XN2, lane);
  {
    const float* xn2 = lds + (helper ? L_XN2 : L_XN);
    float qa[8], qb[8];
#pragma unroll
    for (int k = 0; k < 8; ++k) qa[k] = qb[k] = 0.f;
#pragma unroll
    for (int i = 0; i < WL; ++i) {
      const float xr = lds[L_XN + wave * 4 + r + i * (NW * 4)], xr2 = xn2[wave * 4 + r + i * (NW * 4)];
#pragma unroll
      for (int k = 0; k < 4; ++k) {
        const float w0 = bf_lo(W[i][k]), w1 = bf_hi(W[i][k]);
        qa[2 * k] = fmaf(xr, w0, qa[2 * k]);
        qa[2 * k + 1] = fmaf(xr, w1, qa[2 * k + 1]);
        qb[2 * k] = fmaf(xr2, w0, qb[2 * k]);
        qb[2 * k + 1] = fmaf(xr2, w1, qb[2 * k + 1]);
      }
    }
#pragma unroll
    for (int k = 0; k < 8; ++k) {
      auto s16 = __builtin_amdgcn_permlane16_swap(__float_as_uint(qa[k]), __float_as_uint(qb[k]), false, false);
      const float t = __uint_as_float(s16[0]) + __uint_as_float(s16[1]);
      auto s32 = __builtin_amdgcn_permlane32_swap(__float_as_uint(t), __float_as_uint(t), false, false);
      qa[k] = __uint_as_float(s32[0]) + __uint_as_float(s32[1]);
    }
    if (r == 0 || (r == 1 && helper)) {
      float* qp = lds + (r == 0 ? L_QP : L_QP2) + wave * DH + 8 * c;
      *reinterpret_cast<f32x4*>(qp) = f32x4{qa[0], qa[1], qa[2], qa[3]};
      *reinterpret_cast<f32x4*>(qp + 4) = f32x4{qa[4], qa[5], qa[6], qa[7]};
    }
  }
  __builtin_amdgcn_sched_barrier(0);
  load_tile(A, kp, vp, 1, lo);
  __builtin_amdgcn_sched_barrier(0);
  auto q_final = [&](const float* qp) {
    f32x4 s0 = {0.f, 0.f, 0.f, 0.f}, s1 = {0.f, 0.f, 0.f, 0.f};
#pragma unroll
    for (int w = 0; w < NW; ++w) {
      s0 += *reinterpret_cast<const f32x4*>(qp + w * DH + 8 * c);
      s1 += *reinterpret_cast<const f32x4*>(qp + w * DH + 8 * c + 4);
    }
    return u32x4{pack_bf(s0[0], s0[1]), pack_bf(s0[2], s0[3]), pack_bf(s1[0], s1[1]), pack_bf(s1[2], s1[3])};
  };
  __syncthreads();
  u32x4 q = q_final(lds + L_QP);
  asm volatile("" : "+v"(q));
  if (host) {
    asm volatile("s_waitcnt lgkmcnt(0)" ::: "memory");
    if (lane == 0) ((volatile LAS unsigned*)(lds + L_PG))[wave] = 1u;
  }

  SetT<UF> F, F1;
  if (hostgrp) {
    const int xk = dbl ? 2 * XK : XK;
    const size_t fo = (size_t)(bh * NH + h) * (SK * DH) + (size_t)(SK - NB * xk + xk * b + (xk / NW) * wave + r) * DH + 8 * c;
    load_kv(F, kc + fo, vc + fo);
    if (dbl) load_kv(F1, kc + fo + 4 * UF * DH, vc + fo + 4 * UF * DH);
  }
  auto wave_state_to_lds = [&](float m, float l, float (&acc)[8], float* ac, float* ml) {
    const float mw = xrow_max(m);
    const float sc = __builtin_amdgcn_exp2f(m - mw);
    l = xrow_sum(l * sc);
#pragma unroll
    for (int k = 0; k < 8; ++k) acc[k] = xrow_sum(acc[k] * sc);
    if (r == 0) {
      *reinterpret_cast<f32x4*>(ac + wave * DH + 8 * c) = f32x4{acc[0], acc[1], acc[2], acc[3]};
      *reinterpret_cast<f32x4*>(ac + wave * DH + 8 * c + 4) = f32x4{acc[4], acc[5], acc[6], acc[7]};
      if (c == 0) { ml[2 * wave] = mw; ml[2 * wave + 1] = l; }
    }
  };
  float m = -INFINITY, l = 0.f, acc[8];
#pragma unroll
  for (int k = 0; k < 8; ++k) acc[k] = 0.f;
  compute_tile(Bs, q, m, l, acc);
  load_tile(Bs, kp, vp, 2, lo);
  __builtin_amdgcn_sched_barrier(0);
  compute_tile(A, q, m, l, acc);
  load_tile(A, kp, vp, 3, lo);
  __builtin_amdgcn_sched_barrier(0);
  if (host) {
    compute_tile(F, q, m, l, acc);
    if (dbl) compute_tile(F1, q, m, l, acc);
  } else if (helper) {
    const u32x4 q2 = q_final(lds + L_QP2);
    float m2 = -INFINITY, l2 = 0.f, acc2[8];
#pragma unroll
    for (int k = 0; k < 8; ++k) acc2[k] = 0.f;
    compute_tile(F, q2, m2, l2, acc2);
    if (dbl) compute_tile(F1, q2, m2, l2, acc2);
    wave_state_to_lds(m2, l2, acc2, lds + L_AC2, lds + L_ML2);
    asm volatile("s_waitcnt lgkmcnt(0)" ::: "memory");
    if (lane == 0) ((volatile LAS unsigned*)(lds + L_PG))[wave] = 1u;
  }
  const int tend = TILES - 2 - (host ? (dbl ? 2 * XT : XT) : 0);
  volatile LAS unsigned* wf = (volatile LAS unsigned*)(lds + L_WF);
#define SB __builtin_amdgcn_sched_barrier(0)
#pragma unroll 1
  for (int t = 2; t < tend; t += 2) {
    unsigned tk = 0;
    if (lane == 0) tk = __hip_atomic_fetch_add((LAS unsigned*)(lds + L_TK), 1u, __ATOMIC_RELAXED, __HIP_MEMORY_SCOPE_WORKGROUP);
    const u32x2 ln = *(volatile LAS u32x2*)(lds + L_TK + 2);
    const unsigned t_pre = (unsigned)__builtin_amdgcn_s_memrealtime();
    SB;
    compute_tile(Bs, q, m, l, acc);
    SB;
    {
      asm volatile("" : "+v"(m), "+v"(l), "+v"(acc[0]), "+v"(acc[1]), "+v"(acc[2]), "+v"(acc[3]), "+v"(acc[4]), "+v"(acc[5]), "+v"(acc[6]), "+v"(acc[7]));
      const u32 kk = (u32)__builtin_amdgcn_readfirstlane(tk);
      const u32 due = (u32)__builtin_amdgcn_readfirstlane(ln[0]) + (kk * (u32)__builtin_amdgcn_readfirstlane(ln[1]) >> 8);
      if ((int)(t_pre - due) < 0)
        for (int i = 0; i < 96 && (int)((u32)__builtin_amdgcn_s_memrealtime() - due) < 0; ++i) __builtin_amdgcn_s_sleep(4);
      if (kk == 40u && b == 3 && !host) {
        const u32 now = (u32)__builtin_amdgcn_s_memrealtime();
        if (lane == 0)
          __hip_atomic_store(reinterpret_cast<u32*>(mlg) + 256 + h, (now << 14) | min(now - t_start, 16383u), __ATOMIC_RELAXED, __HIP_MEMORY_SCOPE_AGENT);
      }
    }
    SB;
    load_tile(Bs, kp + (size_t)t * (128 * DH), vp + (size_t)t * (128 * DH), 2, lo);
    SB;
    compute_tile(A, q, m, l, acc);
    SB;
    load_tile(A, kp + (size_t)t * (128 * DH), vp + (size_t)t * (128 * DH), 3, lo);
    SB;
  }
  u32 pw[64];
  while ((wf[0] & wf[1] & wf[2] & wf[3]) == 0u) __builtin_amdgcn_s_sleep(2);
  asm volatile("" ::: "memory");
  project_fetch(lds + L_WO, wave, lane, pw);
  PIN64(pw);
  compute_tile(Bs, q, m, l, acc);
  compute_tile(A, q, m, l, acc);

  wave_state_to_lds(m, l, acc, lds + L_AC, lds + L_ML);
  __syncthreads();
  o_to_lds(lds + L_AC, lds + L_ML, lds + L_O, lane, !host, tid == 0, mlg + (he * NB + b) * 2);
  project_finish(pw, lds + L_O, wave, lane, yA + (size_t)id * DM);
}

constexpr int O_YB = NB * NH * DM, O_YH = O_YB + NFG * NB * DM, O_ML = O_YH + 16 * NB * DM;

__global__ void __launch_bounds__(256) final_sum(const bf16* __restrict__ x, const float* __restrict__ ws, bf16* __restrict__ out) {
  __shared__ __attribute__((aligned(16))) float red[2][4][32];
  const int b = blockIdx.x >> 4, cb = blockIdx.x & 15;
  const int tid = threadIdx.x, c4 = tid & 7, g = tid >> 3;
  const int col = cb * 32 + 4 * c4;
  const f32x4 zero = {0.f, 0.f, 0.f, 0.f};
  const bool split = (g & 1) == 0 && ((g >> 1) & 7) == b, piece = g < 16 && (g & 7) != b;
  const int n = b * DM + cb * 32 + (tid & 31);
  const int ia = (b * NH + g) * DM;
  f32x4 va = *reinterpret_cast<const f32x4*>(ws + (u32)(ia + col));
  f32x4 sb = *reinterpret_cast<const f32x4*>(ws + (u32)((g < NFG ? O_YB + (g * NB + b) * DM : ia) + col));
  const f32x4 vh = *reinterpret_cast<const f32x4*>(ws + (u32)((piece ? O_YH + ((b + (g & 8)) * NB + (g & 7)) * DM : ia) + col));
  const f32x2 ml = *reinterpret_cast<const f32x2*>(ws + (u32)(O_ML + ((b + (tid & 8)) * NB + (tid & 7)) * 2));
  const float xv = __bfloat162float(x[n]);
  bf16* op = out + n;
  asm volatile("" : "+v"(op));
  __builtin_amdgcn_sched_barrier(0);
  float M = ml[0];
  M = fmaxf(M, dpp<0xB1>(M)); M = fmaxf(M, dpp<0x4E>(M)); M = fmaxf(M, dpp<0x141>(M));
  const float e = __builtin_amdgcn_exp2f(ml[0] - M) * ml[1];
  float L = e;
  L += dpp<0xB1>(L); L += dpp<0x4E>(L); L += dpp<0x141>(L);
  const float w = e / L;
  const float wsp = __shfl(w, ((g >> 4) << 3) | b), wp = __shfl(w, g & 15);
  va = split ? va * wsp : va;
  va = piece ? va + vh * wp : va;
  sb = g < NFG ? sb : zero;
#pragma unroll
  for (int k = 0; k < 4; ++k) {
    va[k] = xrow_sum(va[k] + dpp<0x128>(va[k]));
    sb[k] = xrow_sum(sb[k] + dpp<0x128>(sb[k]));
  }
  if ((tid & 63) < 8) {
    *reinterpret_cast<f32x4*>(&red[0][tid >> 6][4 * c4]) = va;
    *reinterpret_cast<f32x4*>(&red[1][tid >> 6][4 * c4]) = sb;
  }
  __syncthreads();
  if (tid < 32) {
    const float a = (red[0][0][tid] + red[0][1][tid]) + (red[0][2][tid] + red[0][3][tid]);
    const float f = (red[1][0][tid] + red[1][1][tid]) + (red[1][2][tid] + red[1][3][tid]);
    *op = __float2bfloat16(rbf(xv + rbf(a)) + rbf(f));
  }
}

extern "C" void kernel_launch(void* const* d_in, const int* in_sizes, int n_in,
                              void* d_out, int out_size, void* d_ws, size_t ws_size,
                              hipStream_t stream) {
  const bf16* x = (const bf16*)d_in[0];
  const bf16* kc = (const bf16*)d_in[1];
  const bf16* vc = (const bf16*)d_in[2];
  const bf16* wq = (const bf16*)d_in[3];
  const bf16* wo = (const bf16*)d_in[4];
  const bf16* wg = (const bf16*)d_in[6];
  const bf16* wu = (const bf16*)d_in[7];
  const bf16* wd = (const bf16*)d_in[8];
  float* ws = (float*)d_ws;
  attn_ffn_main<<<NB * NH, NT, 0, stream>>>(x, kc, vc, wq, wo, wg, wu, wd, ws, ws + O_YB, ws + O_YH, ws + O_ML);
  final_sum<<<NB * 16, 256, 0, stream>>>(x, ws, (bf16*)d_out);
}
```

```cpp
#include <hip/hip_runtime.h>
#include <hip/hip_bf16.h>
#include <stdint.h>

typedef unsigned int u32;
typedef __attribute__((ext_vector_type(4))) unsigned int u32x4;
typedef __attribute__((ext_vector_type(2))) unsigned int u32x2;
typedef __attribute__((ext_vector_type(4))) float f32x4;
typedef __attribute__((ext_vector_type(2))) __bf16 bf16x2;
typedef __hip_bfloat16 bf16;

constexpr int NB = 8, DM = 512, NH = 32, SK = 4096, DH = 128, FF = 1376;
constexpr int NW = 8;
constexpr int U = 4;
constexpr int NFW = 4;
constexpr int NT = (NW + NFW) * 64;
constexpr int TILES = SK / (NW * 4 * U);
constexpr int XK = 64;
constexpr int UF = XK / (NW * 4);
constexpr int XT = NB * XK / (NW * 4 * U);
static_assert(NW == 8 && U == 4 && TILES >= 4 + 2 * XT && (TILES % 2) == 0 && (XT % 2) == 0 && XT * NW * 4 * U == NB * XK, "tiles");

__device__ __forceinline__ float bf_lo(u32 x) { return __uint_as_float(x << 16); }
__device__ __forceinline__ float bf_hi(u32 x) { return __uint_as_float(x & 0xffff0000u); }
__device__ __forceinline__ float rbf(float x) { return __bfloat162float(__float2bfloat16(x)); }
__device__ __forceinline__ u32 pack_bf(float lo, float hi) {
  return (u32)__bfloat16_as_ushort(__float2bfloat16(lo)) | ((u32)__bfloat16_as_ushort(__float2bfloat16(hi)) << 16);
}
__device__ __forceinline__ float dot2(u32 a, u32 b, float acc) {
  return __builtin_amdgcn_fdot2_f32_bf16(__builtin_bit_cast(bf16x2, a), __builtin_bit_cast(bf16x2, b), acc, false);
}
template <int CTRL> __device__ __forceinline__ float dpp(float x) {
  return __builtin_bit_cast(float, __builtin_amdgcn_mov_dpp(__builtin_bit_cast(int, x), CTRL, 0xf, 0xf, true));
}
__device__ __forceinline__ float row16_sum(float x) {
  x += dpp<0xB1>(x);
  x += dpp<0x4E>(x);
  x += dpp<0x141>(x);
  x += dpp<0x140>(x);
  return x;
}
__device__ __forceinline__ float xrow_sum(float x) {
  auto s = __builtin_amdgcn_permlane16_swap(__float_as_uint(x), __float_as_uint(x), false, false);
  x = __uint_as_float(s[0]) + __uint_as_float(s[1]);
  auto t = __builtin_amdgcn_permlane32_swap(__float_as_uint(x), __float_as_uint(x), false, false);
  return __uint_as_float(t[0]) + __uint_as_float(t[1]);
}
__device__ __forceinline__ float xrow_max(float x) {
  auto s = __builtin_amdgcn_permlane16_swap(__float_as_uint(x), __float_as_uint(x), false, false);
  x = fmaxf(__uint_as_float(s[0]), __uint_as_float(s[1]));
  auto t = __builtin_amdgcn_permlane32_swap(__float_as_uint(x), __float_as_uint(x), false, false);
  return fmaxf(__uint_as_float(t[0]), __uint_as_float(t[1]));
}
__device__ __forceinline__ float wave_sum(float x) { return xrow_sum(row16_sum(x)); }
__device__ __forceinline__ u32x4 ld_nt(const bf16* p) { return __builtin_nontemporal_load(reinterpret_cast<const u32x4*>(p)); }
__device__ __forceinline__ u32x4 ld16(const bf16* p) { return *reinterpret_cast<const u32x4*>(p); }
__device__ __forceinline__ u32x4 ld_ffn(const bf16* base, u32 boff) {
  return __builtin_nontemporal_load(reinterpret_cast<const u32x4*>(reinterpret_cast<const char*>(base) + boff));
}

constexpr int L_WO = 0;
constexpr int L_XN = L_WO + DH * DM / 2;
constexpr int L_QP = L_XN + DM;
constexpr int L_UP = L_XN;
constexpr int L_AC = L_UP + NFW * NB * 64;
constexpr int L_ML = L_AC + NW * DH;
constexpr int L_O = L_ML + NW * 2;
constexpr int L_HB = L_O + 2 * DH;
constexpr int L_FL = L_HB + 32 * NB;
constexpr int L_PG = L_FL + 8;
constexpr int L_WF = L_PG + 8;
constexpr int L_QF = L_WF + 4;
constexpr int L_XF = L_QF + 4;
constexpr int L_TK = L_XF + 4;
constexpr int L_XA = L_TK + 4;
constexpr int L_GP = L_XA + 256 * NB;
constexpr int L_END = L_GP + NFW * NB * 64;
static_assert(L_QP + NW * DH <= L_AC, "LDS");
constexpr int L_QP2 = L_QP + NFW * DH, L_AC2 = L_XA, L_ML2 = L_AC2 + NW * DH;
static_assert(L_ML2 + NW * 2 <= L_END && L_END * 4 <= 163840 && (L_XA % 4) == 0 && (L_FL % 4) == 0 && (L_O % 4) == 0, "LDS");
constexpr int NFG = (FF + 63) / 64;
constexpr u32 PSL = 562u * 128u / 5u;
#define LAS __attribute__((address_space(3)))

__device__ __forceinline__ void glds16_nt(const void* gsrc, unsigned lds_dst) {
  unsigned keep;
  asm volatile("s_mov_b32 %0, m0\n\ts_mov_b32 m0, %2\n\ts_nop 0\n\tglobal_load_lds_dwordx4 %1, off nt\n\ts_mov_b32 m0, %0"
               : "=&s"(keep) : "v"(gsrc), "s"(lds_dst) : "memory");
}

#define PIN16(a, o) asm volatile("" : "+v"(a[o]), "+v"(a[o + 1]), "+v"(a[o + 2]), "+v"(a[o + 3]), "+v"(a[o + 4]), "+v"(a[o + 5]), "+v"(a[o + 6]), \
  "+v"(a[o + 7]), "+v"(a[o + 8]), "+v"(a[o + 9]), "+v"(a[o + 10]), "+v"(a[o + 11]), "+v"(a[o + 12]), "+v"(a[o + 13]), "+v"(a[o + 14]), "+v"(a[o + 15]) :: "memory")
#define PIN64(a) do { PIN16(a, 0); PIN16(a, 16); PIN16(a, 32); PIN16(a, 48); } while (0)

template <int CTRL, int N> __device__ __forceinline__ void tr_dpp(float (&acc)[64], bool up) {
#pragma unroll
  for (int k = 0; k < N; ++k) {
    const float keep = up ? acc[k + N] : acc[k];
    const float send = up ? acc[k] : acc[k + N];
    acc[k] = keep + dpp<CTRL>(send);
  }
}
template <int NMIN> __device__ __forceinline__ void treduce(float (&acc)[64], int lane) {
#pragma unroll
  for (int k = 0; k < 32; ++k) {
    auto s = __builtin_amdgcn_permlane32_swap(__float_as_uint(acc[k]), __float_as_uint(acc[k + 32]), false, false);
    acc[k] = __uint_as_float(s[0]) + __uint_as_float(s[1]);
  }
#pragma unroll
  for (int k = 0; k < 16; ++k) {
    auto s = __builtin_amdgcn_permlane16_swap(__float_as_uint(acc[k]), __float_as_uint(acc[k + 16]), false, false);
    acc[k] = __uint_as_float(s[0]) + __uint_as_float(s[1]);
  }
  if (NMIN <= 8) tr_dpp<0x128, 8>(acc, (lane & 8) != 0);
}

template <int U> struct SetT { u32x4 r[2 * U]; };
typedef SetT<U> Set;

template <int U> __device__ __forceinline__ void load_kv(SetT<U>& s, const bf16* kp, const bf16* vp) {
#pragma unroll
  for (int u = 0; u < U; ++u) { s.r[u] = ld_nt(kp + u * 4 * DH); s.r[U + u] = ld_nt(vp + u * 4 * DH); }
}

constexpr int TSZ = 256 * DH;
__device__ __forceinline__ size_t tile_off(int j) { return (size_t)((j & 1) * 16 + (j >> 1) * 64) * DH; }
__device__ __forceinline__ void load_tile(Set& s, const bf16* p, u32 lo) {
#pragma unroll
  for (int j = 0; j < 2 * U; ++j) s.r[j] = ld_ffn(p + tile_off(j), lo);
}
__device__ __forceinline__ float score_tile(const Set& s, const u32x4& q, int lane) {
  float d[8];
#pragma unroll
  for (int j = 0; j < 8; ++j) {
    d[j] = dot2(s.r[j][0], q[0], 0.f);
    d[j] = dot2(s.r[j][1], q[1], d[j]);
    d[j] = dot2(s.r[j][2], q[2], d[j]);
    d[j] = dot2(s.r[j][3], q[3], d[j]);
  }
  const bool b3 = (lane & 8) != 0, b2 = (lane & 4) != 0, b1 = (lane & 2) != 0;
#pragma unroll
  for (int j = 0; j < 4; ++j) d[j] = (b3 ? d[j + 4] : d[j]) + dpp<0x140>(b3 ? d[j] : d[j + 4]);
#pragma unroll
  for (int j = 0; j < 2; ++j) d[j] = (b2 ? d[j + 2] : d[j]) + dpp<0x141>(b2 ? d[j] : d[j + 2]);
  d[0] = (b1 ? d[1] : d[0]) + dpp<0x4E>(b1 ? d[0] : d[1]);
  return d[0] + dpp<0xB1>(d[0]);
}
template <int PAR, int J> __device__ __forceinline__ void pv_load(const Set& s, float p, float (&acc)[8]) {
  const float w = dpp<0x150 + 2 * J + PAR>(p);
#pragma unroll
  for (int k = 0; k < 4; ++k) {
    acc[2 * k] = fmaf(w, bf_lo(s.r[J][k]), acc[2 * k]);
    acc[2 * k + 1] = fmaf(w, bf_hi(s.r[J][k]), acc[2 * k + 1]);
  }
}
template <int PAR> __device__ __forceinline__ void pv_tile(const Set& s, float p, float (&acc)[8]) {
  pv_load<PAR, 0>(s, p, acc); pv_load<PAR, 1>(s, p, acc); pv_load<PAR, 2>(s, p, acc); pv_load<PAR, 3>(s, p, acc);
  pv_load<PAR, 4>(s, p, acc); pv_load<PAR, 5>(s, p, acc); pv_load<PAR, 6>(s, p, acc); pv_load<PAR, 7>(s, p, acc);
}

template <int U> __device__ __forceinline__ void compute_tile(const SetT<U>& s, const u32x4& q, float& m, float& l, float (&acc)[8]) {
  constexpr float LOG2E = 1.4426950408889634f;
  float sc[U];
#pragma unroll
  for (int u = 0; u < U; ++u) {
    float d = dot2(s.r[u][0], q[0], 0.f);
    d = dot2(s.r[u][1], q[1], d);
    d = dot2(s.r[u][2], q[2], d);
    d = dot2(s.r[u][3], q[3], d);
    d = row16_sum(d);
    d = rbf(rbf(d) * 0.08838834764831845f) * LOG2E;
    sc[u] = d;
  }
  float tm = sc[0];
#pragma unroll
  for (int u = 1; u < U; ++u) tm = fmaxf(tm, sc[u]);
  const float mn = fmaxf(m, tm);
  const float alpha = __builtin_amdgcn_exp2f(m - mn);
  m = mn;
  float ps = 0.f;
#pragma unroll
  for (int u = 0; u < U; ++u) { sc[u] = __builtin_amdgcn_exp2f(sc[u] - mn); ps += sc[u]; }
  l = fmaf(l, alpha, ps);
#pragma unroll
  for (int k = 0; k < 8; ++k) acc[k] *= alpha;
#pragma unroll
  for (int u = 0; u < U; ++u) {
#pragma unroll
    for (int k = 0; k < 4; ++k) {
      acc[2 * k] = fmaf(sc[u], bf_lo(s.r[U + u][k]), acc[2 * k]);
      acc[2 * k + 1] = fmaf(sc[u], bf_hi(s.r[U + u][k]), acc[2 * k + 1]);
    }
  }
}

__device__ __forceinline__ void ln_stats(const u32x4& xv, float& mb, float& den) {
  float xf[8];
#pragma unroll
  for (int k = 0; k < 4; ++k) { xf[2 * k] = bf_lo(xv[k]); xf[2 * k + 1] = bf_hi(xv[k]); }
  float s = 0.f;
#pragma unroll
  for (int k = 0; k < 8; ++k) s += xf[k];
  const float mean = wave_sum(s) * (1.f / DM);
  float s2 = 0.f;
#pragma unroll
  for (int k = 0; k < 8; ++k) { const float d = xf[k] - mean; s2 = fmaf(d, d, s2); }
  const float var = wave_sum(s2) * (1.f / DM);
  mb = rbf(mean);
  den = rbf(sqrtf(rbf(rbf(var) + 1e-5f)));
}
__device__ __forceinline__ u32 ln_pair(u32 xp, float mb, float den) { return pack_bf(rbf(bf_lo(xp) - mb) / den, rbf(bf_hi(xp) - mb) / den); }
typedef __attribute__((ext_vector_type(2))) float f32x2;
__device__ __forceinline__ void o_to_lds(const float* ac, const float* ml, float* o, int lane, bool whole, bool writer, float* ml_out) {
  float M = ml[0];
#pragma unroll
  for (int w = 1; w < NW; ++w) M = fmaxf(M, ml[2 * w]);
  float L = 0.f;
  f32x2 o2 = {0.f, 0.f};
#pragma unroll
  for (int w = 0; w < NW; ++w) {
    const float e = __builtin_amdgcn_exp2f(ml[2 * w] - M);
    L = fmaf(e, ml[2 * w + 1], L);
    o2 += e * *reinterpret_cast<const f32x2*>(ac + w * DH + 2 * lane);
  }
  if (!whole && writer) *reinterpret_cast<f32x2*>(ml_out) = f32x2{M, L};
  reinterpret_cast<u32*>(o)[lane] = pack_bf(o2[0] / L, o2[1] / L);
  __builtin_amdgcn_s_waitcnt(0xc07f);
  __builtin_amdgcn_wave_barrier();
}
__device__ __forceinline__ void project_fetch(const float* wo_lds, int sl, int lane, u32 (&pw)[64]) {
  const int rg = lane >> 3;
  const float* wl = wo_lds + (16 * rg) * (DM / 2) + 4 * ((8 * sl + (lane & 7)) ^ (8 * (rg & 1)));
#pragma unroll
  for (int i = 0; i < 8; ++i) {
    const u32x4 w0 = *reinterpret_cast<const u32x4*>(wl + (2 * i) * (DM / 2));
    const u32x4 w1 = *reinterpret_cast<const u32x4*>(wl + (2 * i + 1) * (DM / 2));
#pragma unroll
    for (int k = 0; k < 4; ++k) {
      pw[8 * i + 2 * k] = __builtin_amdgcn_perm(w1[k], w0[k], 0x05040100u);
      pw[8 * i + 2 * k + 1] = __builtin_amdgcn_perm(w1[k], w0[k], 0x07060302u);
    }
  }
}
__device__ __forceinline__ void project_finish(const u32 (&pw)[64], const float* o, int sl, int lane, float* __restrict__ dst) {
  float y[8];
#pragma unroll
  for (int k = 0; k < 8; ++k) y[k] = 0.f;
#pragma unroll
  for (int i4 = 0; i4 < 2; ++i4) {
    const u32x4 ov = *reinterpret_cast<const u32x4*>(o + 8 * (lane >> 3) + 4 * i4);
#pragma unroll
    for (int i = 0; i < 4; ++i)
#pragma unroll
      for (int k = 0; k < 8; ++k) y[k] = dot2(ov[i], pw[8 * (4 * i4 + i) + k], y[k]);
  }
#pragma unroll
  for (int k = 0; k < 4; ++k) {
    auto t = __builtin_amdgcn_permlane32_swap(__float_as_uint(y[k]), __float_as_uint(y[k + 4]), false, false);
    y[k] = __uint_as_float(t[0]) + __uint_as_float(t[1]);
  }
#pragma unroll
  for (int k = 0; k < 2; ++k) {
    auto t = __builtin_amdgcn_permlane16_swap(__float_as_uint(y[k]), __float_as_uint(y[k + 2]), false, false);
    y[k] = __uint_as_float(t[0]) + __uint_as_float(t[1]);
  }
  const bool up = (lane & 8) != 0;
  const float yy = (up ? y[1] : y[0]) + dpp<0x128>(up ? y[0] : y[1]);
  dst[64 * sl + 8 * (lane & 7) + 4 * (lane >> 5) + 2 * ((lane >> 4) & 1) + ((lane >> 3) & 1)] = yy;
}
__device__ __forceinline__ void project(const float* wo_lds, const float* o, int sl, int lane, float* __restrict__ dst) {
  u32 pw[64];
  project_fetch(wo_lds, sl, lane, pw);
  project_finish(pw, o, sl, lane, dst);
}

__global__ void __launch_bounds__(NT) attn_ffn_main(const bf16* __restrict__ x, const bf16* __restrict__ kc,
                                                    const bf16* __restrict__ vc, const bf16* __restrict__ wq,
                                                    const bf16* __restrict__ wo, const bf16* __restrict__ wg,
                                                    const bf16* __restrict__ wu, const bf16* __restrict__ wd,
                                                    float* __restrict__ yA, float* __restrict__ yB,
                                                    float* __restrict__ yH, float* __restrict__ mlg) {
  __shared__ __attribute__((aligned(16))) float lds[L_END];
  const int id = blockIdx.x, b = id >> 5, h = id & 31;
  const int tid = threadIdx.x, lane = tid & 63;
  const int wave = __builtin_amdgcn_readfirstlane(tid >> 6);
  const int r = lane >> 4, c = lane & 15;
  const int he = h >> 1, bh = he & 7;
  const bool hostgrp = (h & 1) == 0, host = hostgrp && b == bh, helper = hostgrp && b != bh, dbl = he < NFG - 16;

  if (wave >= NW) {
    const int fw = wave - NW;
    const u32 t_aux = (u32)__builtin_amdgcn_s_memrealtime();
    __builtin_amdgcn_s_setprio(3);
    {
      const u32x4 xv = ld16(x + b * DM + (u32)(8 * lane));
      const u32x4 xv2 = ld16(x + bh * DM + (u32)(8 * lane));
      const u32 xp = *reinterpret_cast<const u32*>(x + b * DM + 128 * fw + (u32)(2 * lane));
      const u32 xp2 = *reinterpret_cast<const u32*>(x + bh * DM + 128 * fw + (u32)(2 * lane));
      const bf16* wqb = wq + (size_t)(128 * fw) * (NH * DH) + h * DH;
      const u32 wlo = 2u * (u32)(32 * r * (NH * DH) + 8 * c);
      u32x4 W[32];
      __builtin_amdgcn_sched_barrier(0);
#pragma unroll
      for (int i = 0; i < 8; ++i) W[i] = ld_ffn(wqb + (size_t)i * (NH * DH), wlo);
      __builtin_amdgcn_sched_barrier(0);
      __builtin_amdgcn_s_barrier();
      __builtin_amdgcn_sched_barrier(0);
#pragma unroll
      for (int i = 8; i < 32; ++i) {
        W[i] = ld_ffn(wqb + (size_t)i * (NH * DH), wlo);
        __builtin_amdgcn_sched_barrier(0);
        __builtin_amdgcn_s_sleep(4);
        __builtin_amdgcn_sched_barrier(0);
      }
      __builtin_amdgcn_sched_barrier(0);
      {
        float mb, den, mb2, den2;
        ln_stats(xv, mb, den);
        ln_stats(xv2, mb2, den2);
        u32* cw = reinterpret_cast<u32*>(lds + L_XN) + fw * 128 + (lane >> 1) * 4 + (lane & 1);
        cw[0] = ln_pair(xp, mb, den);
        cw[2] = ln_pair(xp2, mb2, den2);
        __builtin_amdgcn_s_waitcnt(0xc07f);
        __builtin_amdgcn_wave_barrier();
      }
      __builtin_amdgcn_sched_barrier(0);
      float qa[8], qb[8];
#pragma unroll
      for (int k = 0; k < 8; ++k) qa[k] = qb[k] = 0.f;
#pragma unroll
      for (int ch = 0; ch < 8; ++ch) {
        const u32x4 cf = *reinterpret_cast<const u32x4*>(lds + L_XN + fw * 128 + (8 * r + ch) * 4);
#pragma unroll
        for (int j = 0; j < 2; ++j) {
          const u32x4 w0 = W[4 * ch + 2 * j], w1 = W[4 * ch + 2 * j + 1];
#pragma unroll
          for (int k = 0; k < 4; ++k) {
            const u32 p0 = __builtin_amdgcn_perm(w1[k], w0[k], 0x05040100u), p1 = __builtin_amdgcn_perm(w1[k], w0[k], 0x07060302u);
            qa[2 * k] = dot2(cf[j], p0, qa[2 * k]);
            qa[2 * k + 1] = dot2(cf[j], p1, qa[2 * k + 1]);
            qb[2 * k] = dot2(cf[2 + j], p0, qb[2 * k]);
            qb[2 * k + 1] = dot2(cf[2 + j], p1, qb[2 * k + 1]);
          }
          __builtin_amdgcn_sched_barrier(0);
        }
      }
#pragma unroll
      for (int k = 0; k < 8; ++k) {
        auto s16 = __builtin_amdgcn_permlane16_swap(__float_as_uint(qa[k]), __float_as_uint(qb[k]), false, false);
        const float t = __uint_as_float(s16[0]) + __uint_as_float(s16[1]);
        auto s32 = __builtin_amdgcn_permlane32_swap(__float_as_uint(t), __float_as_uint(t), false, false);
        qa[k] = __uint_as_float(s32[0]) + __uint_as_float(s32[1]);
      }
      if (r < 2) {
        float* qp = lds + L_QP + (r * NFW + fw) * DH + 8 * c;
        *reinterpret_cast<f32x4*>(qp) = f32x4{qa[0], qa[1], qa[2], qa[3]};
        *reinterpret_cast<f32x4*>(qp + 4) = f32x4{qa[4], qa[5], qa[6], qa[7]};
      }
      asm volatile("s_waitcnt lgkmcnt(0)" ::: "memory");
      if (lane == 0) ((volatile LAS unsigned*)(lds + L_QF))[fw] = 1u;
      __builtin_amdgcn_sched_barrier(0);
    }
    if (fw == 3) { const float tv = yA[(size_t)id * DM + lane]; asm volatile("" :: "v"(tv)); }
    if (host) {
      const u32x4 xs0 = ld16(x + (2 * fw) * DM + (u32)(8 * lane));
      const u32x4 xs1 = ld16(x + (2 * fw + 1) * DM + (u32)(8 * lane));
      auto gate_off = [&](int j) {
        const int c0 = 64 * j + 8 * (lane & 7);
        return 2u * (u32)((128 * fw + 2 * (lane >> 3)) * FF + (c0 < FF ? c0 : c0 - 32));
      };
      const int g0 = (he & 3) * 4 + (he >> 2), g1 = 16 + ((he & 2) ? 2 + (he & 3) : 2 * (he & 3) + (he >> 2));
      u32 goff = gate_off(g0);
      u32x4 w[16];
      auto load_gate = [&]() {
#pragma unroll
        for (int i = 0; i < 16; ++i) w[i] = ld_ffn(wg + (16 * (i >> 1) + (i & 1)) * FF, goff);
      };
      load_gate();
      __builtin_amdgcn_sched_barrier(0);
      {
        float mb0, den0, mb1, den1;
        ln_stats(xs0, mb0, den0);
        ln_stats(xs1, mb1, den1);
#pragma unroll
        for (int k = 0; k < 4; ++k)
          *reinterpret_cast<u32x2*>(lds + L_XA + (4 * lane + k) * NB + 2 * fw) = u32x2{ln_pair(xs0[k], mb0, den0), ln_pair(xs1[k], mb1, den1)};
        asm volatile("s_waitcnt lgkmcnt(0)" ::: "memory");
        volatile LAS unsigned* xf = (volatile LAS unsigned*)(lds + L_XF);
        if (lane == 0) xf[fw] = 1u;
        while ((xf[0] & xf[1] & xf[2] & xf[3]) == 0u) __builtin_amdgcn_s_sleep(1);
        asm volatile("" ::: "memory");
      }
      __builtin_amdgcn_sched_barrier(0);
      float acc[64];
      auto consume = [&](const float* coef, int cstride, bool down, u32 sel_a, u32 sel_b, auto next) {
#pragma unroll
        for (int k = 0; k < 64; ++k) acc[k] = 0.f;
#pragma unroll
        for (int i2 = 0; i2 < 8; ++i2) {
          const u32x4 xa0 = *reinterpret_cast<const u32x4*>(coef + i2 * cstride);
          const u32x4 xa1 = *reinterpret_cast<const u32x4*>(coef + i2 * cstride + 4);
          const u32 sel = i2 < 4 ? sel_a : sel_b, sel2 = sel + (sel == 0x0c0c0c0cu ? 0u : 0x02020202u);
#pragma unroll
          for (int cc = 0; cc < 8; ++cc) {
            const u32 pw = __builtin_amdgcn_perm(w[2 * i2 + 1][cc >> 1], w[2 * i2][cc >> 1], (cc & 1) ? sel2 : sel);
#pragma unroll
            for (int bb = 0; bb < 8; ++bb) {
              const int idx = down ? ((bb >> 2) * 32 + (cc >> 2) * 16 + (bb & 3) * 4 + (cc & 3)) : (bb * 8 + cc);
              acc[idx] = dot2(bb < 4 ? xa0[bb & 3] : xa1[bb & 3], pw, acc[idx]);
            }
            if (cc & 1) __builtin_amdgcn_sched_barrier(0);
          }
          next(2 * i2);
          next(2 * i2 + 1);
          PIN64(acc);
        }
      };
      constexpr u32 SEL = 0x05040100u, SEL0 = 0x0c0c0c0cu;
      const float* xa = lds + L_XA + (64 * fw + (lane >> 3)) * NB;
      volatile LAS unsigned* fl = (volatile LAS unsigned*)(lds + L_FL);
      volatile LAS unsigned* pg = (volatile LAS unsigned*)(lds + L_PG);
      auto ffn_pass = [&](const unsigned pass) __attribute__((always_inline)) {
        const int jj = pass ? g1 : g0;
        consume(xa, 8 * NB, false, SEL, SEL, [&](int i) { w[i] = ld_ffn(wu + (16 * (i >> 1) + (i & 1)) * FF, goff); });
        treduce<8>(acc, lane);
        if (pass) while (min(min(fl[4], fl[5]), min(fl[6], fl[7])) < pass) __builtin_amdgcn_s_sleep(2);
        asm volatile("" ::: "memory");
        *reinterpret_cast<f32x4*>(lds + L_GP + fw * 512 + 8 * lane) = f32x4{acc[0], acc[1], acc[2], acc[3]};
        *reinterpret_cast<f32x4*>(lds + L_GP + fw * 512 + 8 * lane + 4) = f32x4{acc[4], acc[5], acc[6], acc[7]};
        const bool full = 64 * jj + 64 <= FF;
        const u32 doff = 2u * (u32)(2 * (lane >> 4) * DM + 128 * fw + 8 * (lane & 15));
        const bf16* wd0 = wd + (size_t)(64 * jj) * DM;
        const bf16* wd1 = wd0 + (full ? 32 * DM : 0);
        consume(xa, 8 * NB, false, SEL, SEL, [&](int i) { w[i] = ld_ffn((i < 8 ? wd0 : wd1) + (8 * ((i >> 1) & 3) + (i & 1)) * DM, doff); });
        treduce<8>(acc, lane);
        if (!pass) while ((pg[0] & pg[1] & pg[2] & pg[3] & pg[4] & pg[5] & pg[6] & pg[7]) == 0u) __builtin_amdgcn_s_sleep(2);
        asm volatile("" ::: "memory");
        *reinterpret_cast<f32x4*>(lds + L_UP + fw * 512 + 8 * lane) = f32x4{acc[0], acc[1], acc[2], acc[3]};
        *reinterpret_cast<f32x4*>(lds + L_UP + fw * 512 + 8 * lane + 4) = f32x4{acc[4], acc[5], acc[6], acc[7]};
        asm volatile("s_waitcnt lgkmcnt(0)" ::: "memory");
        if (lane == 0) fl[fw] = pass + 1;
        while (min(min(fl[0], fl[1]), min(fl[2], fl[3])) <= pass) __builtin_amdgcn_s_sleep(2);
        asm volatile("" ::: "memory");
        __builtin_amdgcn_sched_barrier(0);
        {
          f32x4 g0 = {0.f, 0.f, 0.f, 0.f}, g1 = g0, u0 = g0, u1 = g0;
#pragma unroll
          for (int v = 0; v < NFW; ++v) {
            g0 += *reinterpret_cast<const f32x4*>(lds + L_GP + v * 512 + 8 * lane);
            g1 += *reinterpret_cast<const f32x4*>(lds + L_GP + v * 512 + 8 * lane + 4);
            u0 += *reinterpret_cast<const f32x4*>(lds + L_UP + v * 512 + 8 * lane);
            u1 += *reinterpret_cast<const f32x4*>(lds + L_UP + v * 512 + 8 * lane + 4);
          }
          const bool cvalid = 64 * jj + 8 * (lane & 7) < FF;
          float hv[8];
#pragma unroll
          for (int k = 0; k < 8; ++k) {
            const float gg = rbf(k < 4 ? g0[k & 3] : g1[k & 3]), uu = rbf(k < 4 ? u0[k & 3] : u1[k & 3]);
            hv[k] = cvalid ? (gg * (gg > 0.f ? 1.f : 0.f)) * uu : 0.f;
          }
          u32* hb = reinterpret_cast<u32*>(lds + L_HB) + (4 * (lane & 7)) * NB + (lane >> 3);
#pragma unroll
          for (int k = 0; k < 4; ++k) hb[k * NB] = pack_bf(hv[2 * k], hv[2 * k + 1]);
          __builtin_amdgcn_s_waitcnt(0xc07f);
          __builtin_amdgcn_wave_barrier();
          asm volatile("" ::: "memory");
          if (lane == 0) fl[4 + fw] = pass + 1;
          __builtin_amdgcn_sched_barrier(0);
        }
        consume(lds + L_HB + (lane >> 4) * NB, 4 * NB, true, SEL, full ? SEL : SEL0, [&](int) {});
        treduce<16>(acc, lane);
        float* yp = yB + (size_t)jj * (NB * DM) + (size_t)(lane >> 5) * (4 * DM) + 128 * fw + 8 * (lane & 15) + 4 * ((lane >> 4) & 1);
#pragma unroll
        for (int bb = 0; bb < 4; ++bb)
          *reinterpret_cast<f32x4*>(yp + bb * DM) = f32x4{acc[bb * 4], acc[bb * 4 + 1], acc[bb * 4 + 2], acc[bb * 4 + 3]};
      };
      ffn_pass(0);
      if (dbl) {
        goff = gate_off(g1);
        load_gate();
        ffn_pass(1);
      }
    }
    if (fw == 0 && !host) {
      for (int i = 0; i < 4096 && (u32)__builtin_amdgcn_s_memrealtime() - t_aux < 1600u; ++i) __builtin_amdgcn_s_sleep(8);
      asm volatile("" ::: "memory");
      const u32 sv = __hip_atomic_load(reinterpret_cast<const u32*>(mlg) + 256 + (lane & 31), __ATOMIC_RELAXED, __HIP_MEMORY_SCOPE_AGENT);
      asm volatile("s_waitcnt vmcnt(0)" ::: "memory");
      const u32 now = (u32)__builtin_amdgcn_s_memrealtime(), el = now - t_aux;
      const u32 age = (now - (sv >> 14)) & 0x3ffffu, d = sv & 16383u;
      const bool ok = lane < 32 && age < el && d > 300u && age + d < el + 300u && age + d + 300u > el;
      const float n = wave_sum(ok ? 1.f : 0.f), sa = wave_sum(ok ? (float)age : 0.f) * (1.f / 30.f);
      const u32 base = n >= 15.f ? now - (u32)sa + 410u - (8u * PSL >> 8) : t_aux + 1180u;
      if (lane == 0) *(volatile LAS u32*)(lds + L_TK + 2) = base;
    }
    {
      const bf16* wop = wo + (size_t)(h * DH + fw * 32) * DM;
      const unsigned wo_lds = (unsigned)(size_t)(LAS float*)(lds + L_WO) + (unsigned)(fw * 32 * DM * 2);
#pragma unroll 1
      for (int i = 0; i < 32; ++i) {
        const u32 due = t_aux + 4100u + (u32)(i * 80);
        for (int j = 0; j < 4096 && (int)((u32)__builtin_amdgcn_s_memrealtime() - due) < 0; ++j) __builtin_amdgcn_s_sleep(4);
        glds16_nt(wop + (size_t)i * DM + (u32)(8 * (lane ^ (8 * (i >> 4)))), wo_lds + i * (DM * 2));
      }
      volatile LAS unsigned* wf = (volatile LAS unsigned*)(lds + L_WF);
      asm volatile("s_waitcnt vmcnt(0)" ::: "memory");
      if (lane == 0) wf[fw] = 1u;
      if (helper) while ((wf[0] & wf[1] & wf[2] & wf[3]) == 0u) __builtin_amdgcn_s_sleep(8);
    }
    if (helper) {
      volatile LAS unsigned* pg = (volatile LAS unsigned*)(lds + L_PG);
      while ((pg[0] & pg[1] & pg[2] & pg[3] & pg[4] & pg[5] & pg[6] & pg[7]) == 0u) __builtin_amdgcn_s_sleep(8);
      asm volatile("" ::: "memory");
      o_to_lds(lds + L_AC2, lds + L_ML2, lds + L_O + DH, lane, false, tid == NW * 64, mlg + (he * NB + b) * 2);
      project(lds + L_WO, lds + L_O + DH, 2 * fw, lane, yH + (size_t)(he * NB + b) * DM);
      project(lds + L_WO, lds + L_O + DH, 2 * fw + 1, lane, yH + (size_t)(he * NB + b) * DM);
    }
    return;
  }

  const unsigned t_start = (unsigned)__builtin_amdgcn_s_memrealtime();
  Set A, Bs, C;
  const bf16* kp = kc + (size_t)id * (SK * DH) + (size_t)wave * DH;
  const bf16* vp = vc + (size_t)id * (SK * DH) + (size_t)wave * DH;
  const u32 lo = 2u * (u32)(((r & 1) * 8 + (r >> 1) * 32) * DH + 8 * c);
  const int nk = TILES / 2 - (host ? (dbl ? XT : XT / 2) : 0);
  const int R0 = 8 * (h & 1);
  auto po = [&](int t) { const int v = t + R0; return (size_t)(v >= nk ? v - nk : v) * TSZ; };
  if (tid < L_XA - L_FL)
    reinterpret_cast<u32*>(lds)[L_FL + tid] = tid == L_TK + 2 - L_FL ? t_start + (host ? 100u : 1100u) : tid == L_TK + 3 - L_FL ? (host ? 50u * 256u : PSL) : tid == L_TK - L_FL ? 8u : 0u;
  __builtin_amdgcn_sched_barrier(0);
#pragma unroll
  for (int j = 0; j < 2; ++j) C.r[j] = ld_ffn(kp + po(0) + tile_off(j), lo);
  __builtin_amdgcn_sched_barrier(0);
  asm volatile("s_waitcnt lgkmcnt(0)" ::: "memory");
  __builtin_amdgcn_s_barrier();
  __builtin_amdgcn_sched_barrier(0);
#pragma unroll
  for (int j = 2; j < 2 * U; ++j) C.r[j] = ld_ffn(kp + po(0) + tile_off(j), lo);
  SetT<UF> F, F1;
  if (hostgrp) {
    const int xk = dbl ? 2 * XK : XK;
    const size_t fo = (size_t)(bh * NH + h) * (SK * DH) + (size_t)(SK - NB * xk + xk * b + (xk / NW) * wave + r) * DH + 8 * c;
    load_kv(F, kc + fo, vc + fo);
    if (dbl) load_kv(F1, kc + fo + 4 * UF * DH, vc + fo + 4 * UF * DH);
  }
  __builtin_amdgcn_sched_barrier(0);
  load_tile(Bs, kp + po(0) + TSZ, lo);
  __builtin_amdgcn_sched_barrier(0);
  load_tile(A, kp + po(2), lo);
  __builtin_amdgcn_sched_barrier(0);
  auto q_final = [&](const float* qp) {
    const f32x4 s0 = *reinterpret_cast<const f32x4*>(qp + r * DH + 8 * c), s1 = *reinterpret_cast<const f32x4*>(qp + r * DH + 8 * c + 4);
    float t[8];
#pragma unroll
    for (int k = 0; k < 8; ++k) t[k] = xrow_sum(k < 4 ? s0[k & 3] : s1[k & 3]);
    return u32x4{pack_bf(t[0], t[1]), pack_bf(t[2], t[3]), pack_bf(t[4], t[5]), pack_bf(t[6], t[7])};
  };
  auto wave_state_to_lds = [&](float m, float l, float (&acc)[8], float* ac, float* ml) {
    const float mw = xrow_max(m);
    const float sc = __builtin_amdgcn_exp2f(m - mw);
    l = xrow_sum(l * sc);
#pragma unroll
    for (int k = 0; k < 8; ++k) acc[k] = xrow_sum(acc[k] * sc);
    if (r == 0) {
      *reinterpret_cast<f32x4*>(ac + wave * DH + 8 * c) = f32x4{acc[0], acc[1], acc[2], acc[3]};
      *reinterpret_cast<f32x4*>(ac + wave * DH + 8 * c + 4) = f32x4{acc[4], acc[5], acc[6], acc[7]};
      if (c == 0) { ml[2 * wave] = mw; ml[2 * wave + 1] = l; }
    }
  };
  {
    volatile LAS unsigned* qf = (volatile LAS unsigned*)(lds + L_QF);
    while ((qf[0] & qf[1] & qf[2] & qf[3]) == 0u) __builtin_amdgcn_s_sleep(1);
    asm volatile("" ::: "memory");
  }
  float m = -INFINITY, l = 0.f, acc[8];
#pragma unroll
  for (int k = 0; k < 8; ++k) acc[k] = 0.f;
  if (hostgrp) {
    const u32x4 q2 = q_final(lds + L_QP2);
    compute_tile(F, q2, m, l, acc);
    if (dbl) compute_tile(F1, q2, m, l, acc);
    if (helper) {
      wave_state_to_lds(m, l, acc, lds + L_AC2, lds + L_ML2);
      m = -INFINITY; l = 0.f;
#pragma unroll
      for (int k = 0; k < 8; ++k) acc[k] = 0.f;
    }
  }
  __builtin_amdgcn_sched_barrier(0);
  u32x4 q = q_final(lds + L_QP);
  asm volatile("" : "+v"(q));
  if (hostgrp) {
    asm volatile("s_waitcnt lgkmcnt(0)" ::: "memory");
    if (lane == 0) ((volatile LAS unsigned*)(lds + L_PG))[wave] = 1u;
  }
#define SB __builtin_amdgcn_sched_barrier(0)
  float sc[8];
#pragma unroll
  for (int k = 0; k < 8; ++k) sc[k] = -INFINITY;
  auto push = [&](float s0, float s1) {
#pragma unroll
    for (int k = 7; k > 0; --k) sc[k] = sc[k - 1];
    sc[0] = rbf(rbf((lane & 1) ? s1 : s0) * 0.08838834764831845f) * 1.4426950408889634f;
  };
  {
    const float s0 = score_tile(C, q, lane);
    SB;
    load_tile(C, kp + po(2) + TSZ, lo);
    SB;
    const float s1 = score_tile(Bs, q, lane);
    SB;
    load_tile(Bs, kp + po(4), lo);
    SB;
    push(s0, s1);
    const float s2 = score_tile(A, q, lane);
    SB;
    load_tile(A, kp + po(4) + TSZ, lo);
    SB;
    const float s3 = score_tile(C, q, lane);
    push(s2, s3);
    SB;
  }
  volatile LAS unsigned* wf = (volatile LAS unsigned*)(lds + L_WF);
  unsigned tk, t_pre;
  u32x2 ln;
  auto pace_fetch = [&]() __attribute__((always_inline)) {
    tk = 0;
    if (lane == 0) tk = __hip_atomic_fetch_add((LAS unsigned*)(lds + L_TK), 1u, __ATOMIC_RELAXED, __HIP_MEMORY_SCOPE_WORKGROUP);
    ln = *(volatile LAS u32x2*)(lds + L_TK + 2);
    t_pre = (unsigned)__builtin_amdgcn_s_memrealtime();
  };
  auto pace_wait = [&]() __attribute__((always_inline)) {
    const u32 kk = (u32)__builtin_amdgcn_readfirstlane(tk);
    const u32 due = (u32)__builtin_amdgcn_readfirstlane(ln[0]) + (kk * (u32)__builtin_amdgcn_readfirstlane(ln[1]) >> 8);
    if (kk == 8u && b == 3 && !host) {
      if (lane == 0)
        __hip_atomic_store(reinterpret_cast<u32*>(mlg) + 256 + h, (t_pre << 14) | min(t_pre - t_start, 16383u), __ATOMIC_RELAXED, __HIP_MEMORY_SCOPE_AGENT);
    }
    if ((int)(t_pre - due) < 0)
      for (int i = 0; i < 96 && (int)((u32)__builtin_amdgcn_s_memrealtime() - due) < 0; ++i) __builtin_amdgcn_s_sleep(4);
  };
#pragma unroll 1
  for (int t = 4; t < nk; t += 2) {
    pace_fetch();
    SB;
    float s0 = score_tile(Bs, q, lane);
    SB;
    asm volatile("" : "+v"(s0));
    pace_wait();
    SB;
    const bf16* np = t + 2 < nk ? kp + po(t + 2) : vp + po(t);
    load_tile(Bs, np, lo);
    SB;
    const float s1 = score_tile(A, q, lane);
    SB;
    load_tile(A, np + TSZ, lo);
    push(s0, s1);
    SB;
  }
  {
    float mx = m;
#pragma unroll
    for (int k = 0; k < 8; ++k) mx = fmaxf(mx, sc[k]);
    mx = fmaxf(mx, dpp<0xB1>(mx)); mx = fmaxf(mx, dpp<0x4E>(mx)); mx = fmaxf(mx, dpp<0x141>(mx)); mx = fmaxf(mx, dpp<0x140>(mx));
    const float alpha = __builtin_amdgcn_exp2f(m - mx);
    m = mx;
    float ps = 0.f;
#pragma unroll
    for (int k = 0; k < 8; ++k) { sc[k] = __builtin_amdgcn_exp2f(sc[k] - mx); ps += sc[k]; acc[k] *= alpha; }
    l = fmaf(l, alpha, row16_sum(ps));
  }
  SB;
#pragma unroll 1
  for (int t = nk - 4; t >= 0; t -= 2) {
    pace_fetch();
    SB;
    pv_tile<0>(Bs, sc[0], acc);
    SB;
    asm volatile("" : "+v"(acc[0]), "+v"(acc[1]), "+v"(acc[2]), "+v"(acc[3]), "+v"(acc[4]), "+v"(acc[5]), "+v"(acc[6]), "+v"(acc[7]));
    pace_wait();
    SB;
    const bf16* np = vp + po(t);
    load_tile(Bs, np, lo);
    SB;
    pv_tile<1>(A, sc[0], acc);
    SB;
    load_tile(A, np + TSZ, lo);
#pragma unroll
    for (int k = 0; k < 7; ++k) sc[k] = sc[k + 1];
    SB;
  }
  u32 pw[64];
  while ((wf[0] & wf[1] & wf[2] & wf[3]) == 0u) __builtin_amdgcn_s_sleep(2);
  asm volatile("" ::: "memory");
  project_fetch(lds + L_WO, wave, lane, pw);
  PIN64(pw);
  __builtin_amdgcn_sched_barrier(0);
  pv_tile<0>(Bs, sc[0], acc);
  pv_tile<1>(A, sc[0], acc);

  wave_state_to_lds(m, l, acc, lds + L_AC, lds + L_ML);
  __syncthreads();
  o_to_lds(lds + L_AC, lds + L_ML, lds + L_O, lane, !host, tid == 0, mlg + (he * NB + b) * 2);
  project_finish(pw, lds + L_O, wave, lane, yA + (size_t)id * DM);
}

constexpr int O_YB = NB * NH * DM, O_YH = O_YB + NFG * NB * DM, O_ML = O_YH + 16 * NB * DM;

__global__ void __launch_bounds__(256) final_sum(const bf16* __restrict__ x, const float* __restrict__ ws, bf16* __restrict__ out) {
  __shared__ __attribute__((aligned(16))) float red[2][4][32];
  const int b = blockIdx.x >> 4, cb = blockIdx.x & 15;
  const int tid = threadIdx.x, c4 = tid & 7, g = tid >> 3;
  const int col = cb * 32 + 4 * c4;
  const f32x4 zero = {0.f, 0.f, 0.f, 0.f};
  const bool split = (g & 1) == 0 && ((g >> 1) & 7) == b, piece = g < 16 && (g & 7) != b;
  const int n = b * DM + cb * 32 + (tid & 31);
  const int ia = (b * NH + g) * DM;
  f32x4 va = *reinterpret_cast<const f32x4*>(ws + (u32)(ia + col));
  f32x4 sb = *reinterpret_cast<const f32x4*>(ws + (u32)((g < NFG ? O_YB + (g * NB + b) * DM : ia) + col));
  const f32x4 vh = *reinterpret_cast<const f32x4*>(ws + (u32)((piece ? O_YH + ((b + (g & 8)) * NB + (g & 7)) * DM : ia) + col));
  const f32x2 ml = *reinterpret_cast<const f32x2*>(ws + (u32)(O_ML + ((b + (tid & 8)) * NB + (tid & 7)) * 2));
  const float xv = __bfloat162float(x[n]);
  bf16* op = out + n;
  asm volatile("" : "+v"(op));
  __builtin_amdgcn_sched_barrier(0);
  float M = ml[0];
  M = fmaxf(M, dpp<0xB1>(M)); M = fmaxf(M, dpp<0x4E>(M)); M = fmaxf(M, dpp<0x141>(M));
  const float e = __builtin_amdgcn_exp2f(ml[0] - M) * ml[1];
  float L = e;
  L += dpp<0xB1>(L); L += dpp<0x4E>(L); L += dpp<0x141>(L);
  const float w = e / L;
  const float wsp = __shfl(w, ((g >> 4) << 3) | b), wp = __shfl(w, g & 15);
  va = split ? va * wsp : va;
  va = piece ? va + vh * wp : va;
  sb = g < NFG ? sb : zero;
#pragma unroll
  for (int k = 0; k < 4; ++k) {
    va[k] = xrow_sum(va[k] + dpp<0x128>(va[k]));
    sb[k] = xrow_sum(sb[k] + dpp<0x128>(sb[k]));
  }
  if ((tid & 63) < 8) {
    *reinterpret_cast<f32x4*>(&red[0][tid >> 6][4 * c4]) = va;
    *reinterpret_cast<f32x4*>(&red[1][tid >> 6][4 * c4]) = sb;
  }
  __syncthreads();
  if (tid < 32) {
    const float a = (red[0][0][tid] + red[0][1][tid]) + (red[0][2][tid] + red[0][3][tid]);
    const float f = (red[1][0][tid] + red[1][1][tid]) + (red[1][2][tid] + red[1][3][tid]);
    *op = __float2bfloat16(rbf(xv + rbf(a)) + rbf(f));
  }
}

extern "C" void kernel_launch(void* const* d_in, const int* in_sizes, int n_in,
                              void* d_out, int out_size, void* d_ws, size_t ws_size,
                              hipStream_t stream) {
  const bf16* x = (const bf16*)d_in[0];
  const bf16* kc = (const bf16*)d_in[1];
  const bf16* vc = (const bf16*)d_in[2];
  const bf16* wq = (const bf16*)d_in[3];
  const bf16* wo = (const bf16*)d_in[4];
  const bf16* wg = (const bf16*)d_in[6];
  const bf16* wu = (const bf16*)d_in[7];
  const bf16* wd = (const bf16*)d_in[8];
  float* ws = (float*)d_ws;
  attn_ffn_main<<<NB * NH, NT, 0, stream>>>(x, kc, vc, wq, wo, wg, wu, wd, ws, ws + O_YB, ws + O_YH, ws + O_ML);
  final_sum<<<NB * 16, 256, 0, stream>>>(x, ws, (bf16*)d_out);
}
```
